```python
import torch

def _softmax(x, axis=-1):
    return torch.softmax(x, dim=axis)

def _gelu(x):
    return torch.nn.functional.gelu(x, approximate='tanh')

def _silu(x):
    return torch.nn.functional.silu(x)

def _sigmoid(x):
    return torch.sigmoid(x)

def _layernorm(x, axis=-1, eps=1e-05):
    m = x.mean(dim=axis, keepdim=True)
    v = x.var(dim=axis, keepdim=True, unbiased=False)
    return (x - m) / torch.sqrt(v + eps)

def _scatter_add(tgt, idx, src, axis=0):
    return tgt.clone().index_add_(axis, idx, src)

def _segment_sum(src, seg, n):
    out = torch.zeros((n,) + src.shape[1:], dtype=src.dtype)
    return out.index_add_(0, seg, src)

def _pad(x, pw):
    flat = tuple((v for pair in reversed(pw) for v in pair))
    return torch.nn.functional.pad(x, flat)
kernel_name = 'recurrent_scan__memory_aug_recurrent_mem_tr__glu_gated_bilinear__prompt'

def setup_inputs():

    _w = lambda a: (lambda t: t.to(torch.bfloat16) if t.is_floating_point() else t)(torch.as_tensor(a))

    def _A_setup():
        B, S, Dm = (1, 2048, 512)
        H, Dh = (32, 128)
        M, seg = (8, 32)
        seg = min(seg, S)
        S = S // seg * seg
        return {'x': _w((lambda *s: torch.randn(*s, dtype=torch.bfloat16) * 0.1)(B, S, Dm)), 'mem0': _w((lambda *s: torch.randn(*s, dtype=torch.bfloat16) * 1.6)(B, M, Dm)), 'Wq': _w((lambda *s: torch.randn(*s, dtype=torch.bfloat16) * 0.8)(Dm, H, Dh) * Dm ** (-0.5)), 'Wk': _w((lambda *s: torch.randn(*s, dtype=torch.bfloat16) * 0.8)(Dm, H, Dh) * Dm ** (-0.5)), 'Wv': _w((lambda *s: torch.randn(*s, dtype=torch.bfloat16) * 1.6)(Dm, H, Dh) * Dm ** (-0.5)), 'Wo': _w((lambda *s: torch.randn(*s, dtype=torch.bfloat16) * 1.6)(H, Dh, Dm) * (H * Dh) ** (-0.5)), 'seg_len': seg}
    _A_in = _A_setup()

    def _B_setup():
        B, S, D, F = (1, 2048, 512, 1792)
        return {'x': _w((lambda *s: torch.randn(*s, dtype=torch.bfloat16) * 0.1)(B, S, D)), 'w_gate': _w((lambda *s: torch.randn(*s, dtype=torch.bfloat16) * 0.4)(D, F) * D ** (-0.5)), 'w_up': _w((lambda *s: torch.randn(*s, dtype=torch.bfloat16) * 0.4)(D, F) * D ** (-0.5)), 'w_down': _w((lambda *s: torch.randn(*s, dtype=torch.bfloat16) * 0.4)(F, D) * F ** (-0.5))}
    _B_in = _B_setup()
    return {'A_x': _A_in['x'], 'A_mem0': _A_in['mem0'], 'A_Wq': _A_in['Wq'], 'A_Wk': _A_in['Wk'], 'A_Wv': _A_in['Wv'], 'A_Wo': _A_in['Wo'], 'A_seg_len': _A_in['seg_len'], 'B_x': _B_in['x'], 'B_w_gate': _B_in['w_gate'], 'B_w_up': _B_in['w_up'], 'B_w_down': _B_in['w_down']}

def reference(A_x, A_mem0, A_Wq, A_Wk, A_Wv, A_Wo, A_seg_len, B_x, B_w_gate, B_w_up, B_w_down):

    def _A_ref(x, mem0, Wq, Wk, Wv, Wo, seg_len):
        B, S, Dm = x.shape
        H, Dh = (Wq.shape[1], Wq.shape[2])
        M = mem0.shape[1]
        scale = Dh ** (-0.5)
        n_seg = S // seg_len
        L = M + seg_len
        idx = torch.arange(L)
        mask = (idx[:, None] < M) | (idx[:, None] >= idx[None, :])
        mem = mem0
        outs = []
        for s in range(n_seg):
            xs = x[:, s * seg_len:(s + 1) * seg_len, :].clone()
            h = torch.concatenate([mem, xs], axis=1)
            q = torch.einsum('btd,dhe->bhte', h, Wq)
            k = torch.einsum('btd,dhe->bhte', h, Wk)
            v = torch.einsum('btd,dhe->bhte', h, Wv)
            scores = torch.einsum('bhqe,bhke->bhqk', q, k) * scale
            scores = torch.where(mask[None, None], scores, -1000000000.0)
            p = _softmax(scores, axis=-1)
            o = torch.einsum('bhqk,bhke->bhqe', p, v)
            o = torch.einsum('bhte,hed->btd', o, Wo)
            mem = o[:, :M, :].clone()
            outs.append(o[:, M:, :])
        return torch.concatenate(outs, axis=1)

    def _B_ref(x, w_gate, w_up, w_down):
        g = torch.einsum('bsd,df->bsf', x, w_gate)
        u = torch.einsum('bsd,df->bsf', x, w_up)
        h = g * u
        return torch.einsum('bsf,fd->bsd', h, w_down)
    x = A_x
    xn = _layernorm(x, axis=-1)
    s = _A_ref(xn, A_mem0, A_Wq, A_Wk, A_Wv, A_Wo, A_seg_len)
    g = _silu(_B_ref(xn, B_w_gate, B_w_up, B_w_down))
    return x + s * g
```

```cpp
#include <hip/hip_runtime.h>
#include <hip/hip_bf16.h>
#include <stdint.h>

typedef __bf16 bf8 __attribute__((ext_vector_type(8)));
typedef __bf16 bf2 __attribute__((ext_vector_type(2)));
typedef short s4 __attribute__((ext_vector_type(4)));
typedef float f4 __attribute__((ext_vector_type(4)));
typedef float f2 __attribute__((ext_vector_type(2)));
typedef unsigned u4 __attribute__((ext_vector_type(4)));
typedef unsigned u2 __attribute__((ext_vector_type(2)));
#define LAS __attribute__((address_space(3)))
#define GAS __attribute__((address_space(1)))
typedef LAS char* lptr;
#define DEVI __device__ __forceinline__

constexpr int NSEG = 64, FF = 1792;
constexpr unsigned WS_COMM = 0;
constexpr unsigned COMM_CTRL = 192 * 16384;
constexpr unsigned CTRL_BYTES = 32768;
constexpr unsigned WS_ROOT = COMM_CTRL + CTRL_BYTES;
constexpr int F_XN = 64, F_O = 128  , F_KV = 4096, F_Q = F_KV + 2048;
constexpr unsigned WS_OC = COMM_CTRL + 0x60000;
constexpr unsigned WS_KMF = COMM_CTRL + 65536;
DEVI constexpr unsigned kmf_off(unsigned s, unsigned h) { return WS_KMF + h * 10240 + (s + 2) * 128; }
constexpr unsigned WS_XN = 7u << 20;
constexpr unsigned WS_G = 5u << 20;
constexpr unsigned WS_KM = 84u << 20;
DEVI constexpr unsigned km_off(unsigned s, unsigned h) { return WS_KM + h * (80 * 4096) + (s + 1) * 4096; }
constexpr unsigned WS_H = 12u << 20;
constexpr unsigned WS_KX = 20u << 20;
DEVI constexpr unsigned kx_off(unsigned s, unsigned h) { return WS_KX + h * (1u << 20) + s * 16384; }
constexpr unsigned WS_QX = 52u << 20;
constexpr unsigned WS_O = 68u << 20;
constexpr unsigned WS_END = 94u << 20;
constexpr int LDS_BYTES = 160 * 1024;

struct Params {
    const char* x; const char* mem0; const char* wq; const char* wk; const char* wv; const char* wo;
    const char* wg; const char* wu; const char* wd; char* out; char* ws;
    int d1; int d2;
};

DEVI unsigned pk2(float a, float b) { f2 v = {a, b}; return __builtin_bit_cast(unsigned, __builtin_convertvector(v, bf2)); }
DEVI float bfr(float a) { return (float)(__bf16)a; }
DEVI float bflo(unsigned u) { return __builtin_bit_cast(float, u << 16); }
DEVI float bfhi(unsigned u) { return __builtin_bit_cast(float, u & 0xffff0000u); }
DEVI s4 trd(lptr p) { return __builtin_amdgcn_ds_read_tr16_b64_v4i16((LAS s4*)p); }
DEVI bf8 cat2(s4 a, s4 b) { return __builtin_bit_cast(bf8, __builtin_shufflevector(a, b, 0, 1, 2, 3, 4, 5, 6, 7)); }
DEVI bf8 asbf(u4 v) { return __builtin_bit_cast(bf8, v); }
DEVI u4 ldsr(lptr p) { return *(LAS u4*)p; }
DEVI f4 mm32(bf8 a, bf8 b, f4 c) { return __builtin_amdgcn_mfma_f32_16x16x32_bf16(a, b, c, 0, 0, 0); }
#define WAIT_VM(n) asm volatile("s_waitcnt vmcnt(" #n ")" ::: "memory")
#define WAIT_LGKM0() asm volatile("s_waitcnt lgkmcnt(0)" ::: "memory")
#define BAR() __builtin_amdgcn_s_barrier()
#define LBAR() do { WAIT_LGKM0(); BAR(); } while (0)
DEVI void glds16(const char* g, lptr l) { __builtin_amdgcn_global_load_lds((const GAS unsigned*)g, (LAS unsigned*)l, 16, 0, 0); }
DEVI float u2f(unsigned u) { return __builtin_bit_cast(float, u); }
DEVI unsigned f2u(float f) { return __builtin_bit_cast(unsigned, f); }
DEVI float xmax16(float m) { auto r = __builtin_amdgcn_permlane16_swap(f2u(m), f2u(m), false, false); return __builtin_elementwise_maximum(u2f(r[0]), u2f(r[1])); }
DEVI float xmax32(float m) { auto r = __builtin_amdgcn_permlane32_swap(f2u(m), f2u(m), false, false); return __builtin_elementwise_maximum(u2f(r[0]), u2f(r[1])); }

typedef __amdgpu_buffer_rsrc_t rsrc_t;
DEVI rsrc_t mkrsrc(const void* p, unsigned bytes) { return __builtin_amdgcn_make_buffer_rsrc((void*)p, 0, bytes, 0x00020000); }
template <int ROWB, class F> DEVI void stage_unit_b(rsrc_t r, F off, int iota0, lptr dst, int lane) {
    constexpr int LPR = ROWB / 16, RPI = 64 / LPR;
#pragma unroll
    for (int i = 0; i < 8; ++i) {
        const int rl = i * RPI + lane / LPR, cp = lane % LPR;
        const int sw = ROWB == 64 ? ((rl >> 2) & 1) : ((rl >> 1) & 3);
        __builtin_amdgcn_raw_ptr_buffer_load_lds(r, (LAS void*)(dst + i * 1024), 16, off(iota0 + rl, cp ^ (sw << 1)), 0, 0, 0);
        __builtin_amdgcn_sched_barrier(0);
    }
}
template <int ROWB> DEVI int tr_base(int ct, int lane) {
    const int sw = ROWB == 64 ? ((lane >> 4) & 1) : ((lane >> 3) & 3);
    return (lane >> 2) * ROWB + ((ct ^ sw) * 32) + 8 * (lane & 3);
}

constexpr int SPIN_CAP = 1 << 19;
DEVI u4 ldw(rsrc_t r, unsigned voff, unsigned soff) { return __builtin_amdgcn_raw_buffer_load_b128(r, voff, soff, 16); }
DEVI void stw(u4 v, rsrc_t r, unsigned voff, unsigned soff) { __builtin_amdgcn_raw_buffer_store_b128(v, r, voff, soff, 16); }
DEVI GAS unsigned* flagp(const Params& p, int i) { return (GAS unsigned*)(p.ws + WS_COMM + COMM_CTRL) + i; }
DEVI bool wait_flag(const Params& p, int i, unsigned want) {
    GAS unsigned* f = flagp(p, i);
    for (int sp = 0; sp < SPIN_CAP; ++sp) {
        if ((unsigned)__builtin_amdgcn_readfirstlane(__hip_atomic_load(f, __ATOMIC_RELAXED, __HIP_MEMORY_SCOPE_AGENT)) >= want) { asm volatile("" ::: "memory"); return true; }
        __builtin_amdgcn_s_sleep(4);
    }
    return false;
}
DEVI bool wait_km(const Params& p, int s, int h, int nap) {
    GAS unsigned* f = (GAS unsigned*)(p.ws + kmf_off(s, h));
    for (int sp = 0; sp < SPIN_CAP; ++sp) {
        if (__builtin_amdgcn_ballot_w64(__hip_atomic_load(f, __ATOMIC_RELAXED, __HIP_MEMORY_SCOPE_AGENT) == ~0u) == 0) { asm volatile("" ::: "memory"); return true; }
        if (nap) __builtin_amdgcn_s_sleep(24); else __builtin_amdgcn_s_sleep(2);
    }
    return false;
}
typedef unsigned long long u64;
DEVI GAS u64* rootp(const Params& p, int byte) { return (GAS u64*)(p.ws + WS_ROOT + byte); }
DEVI void add_flag(const Params& p, int i, int lane);
DEVI bool wait_go(const Params& p, u64 tag, int lane) {
    if (tag == 0x9e3779b97f4a7c15ull) __builtin_amdgcn_s_sleep(1);
    __builtin_amdgcn_s_barrier();
    if (threadIdx.x < 64) {
        GAS u64* f = rootp(p, 128 + 128 * (blockIdx.x & 7));
        for (int sp = 0;; ++sp) {
            const u64 v = __hip_atomic_load(f, __ATOMIC_RELAXED, __HIP_MEMORY_SCOPE_AGENT);
            if (__builtin_amdgcn_ballot_w64(((v >> 3) ^ tag) << 3 != 0) == 0) break;
            if (sp > SPIN_CAP) return false;
            __builtin_amdgcn_s_sleep(8);
        }
        add_flag(p, 1, lane);
    }
    asm volatile("" ::: "memory");
    __builtin_amdgcn_s_barrier();
    asm volatile("" ::: "memory");
    return true;
}
DEVI void add_flag(const Params& p, int i, int lane) { if (lane == 0) __hip_atomic_fetch_add(flagp(p, i), 1u, __ATOMIC_RELAXED, __HIP_MEMORY_SCOPE_AGENT); }
#define SB() __builtin_amdgcn_sched_barrier(0)

DEVI void ln_norm(u4 (&v)[4]) {
    float s = 0.f;
#pragma unroll
    for (int j = 0; j < 4; ++j)
#pragma unroll
        for (int q = 0; q < 4; ++q) s += bflo(v[j][q]) + bfhi(v[j][q]);
    s += __shfl_xor(s, 4); s += __shfl_xor(s, 8); s += __shfl_xor(s, 16); s += __shfl_xor(s, 32);
    const float mean = s * (1.f / 512.f);
    float q2 = 0.f;
#pragma unroll
    for (int j = 0; j < 4; ++j)
#pragma unroll
        for (int q = 0; q < 4; ++q) { float a = bflo(v[j][q]) - mean, b = bfhi(v[j][q]) - mean; q2 += a * a + b * b; }
    q2 += __shfl_xor(q2, 4); q2 += __shfl_xor(q2, 8); q2 += __shfl_xor(q2, 16); q2 += __shfl_xor(q2, 32);
    const float mb = bfr(mean), vb = bfr(q2 * (1.f / 512.f));
    const float rd = 1.f / bfr(sqrtf(bfr(vb + 1e-5f)));
#pragma unroll
    for (int j = 0; j < 4; ++j)
#pragma unroll
        for (int q = 0; q < 4; ++q) v[j][q] = pk2(bfr(bflo(v[j][q]) - mb) * rd, bfr(bfhi(v[j][q]) - mb) * rd);
}
DEVI float wave_sum(float v) {
#define DPP_ADD(ctl, rm) v += u2f(__builtin_amdgcn_update_dpp(0, (int)f2u(v), ctl, rm, 0xf, true))
    DPP_ADD(0x111, 0xf); DPP_ADD(0x112, 0xf); DPP_ADD(0x114, 0xf); DPP_ADD(0x118, 0xf); DPP_ADD(0x142, 0xa); DPP_ADD(0x143, 0xc);
#undef DPP_ADD
    return u2f(__builtin_amdgcn_readlane((int)f2u(v), 63));
}
DEVI u4 ln_row64(u4 v) {
    float s = 0.f;
#pragma unroll
    for (int q = 0; q < 4; ++q) s += bflo(v[q]) + bfhi(v[q]);
    const float mean = wave_sum(s) * (1.f / 512.f);
    float q2 = 0.f;
#pragma unroll
    for (int q = 0; q < 4; ++q) { float a = bflo(v[q]) - mean, b = bfhi(v[q]) - mean; q2 += a * a + b * b; }
    const float mb = bfr(mean), vb = bfr(wave_sum(q2) * (1.f / 512.f));
    const float rd = 1.f / bfr(sqrtf(bfr(vb + 1e-5f)));
#pragma unroll
    for (int q = 0; q < 4; ++q) v[q] = pk2(bfr(bflo(v[q]) - mb) * rd, bfr(bfhi(v[q]) - mb) * rd);
    return v;
}
DEVI unsigned ln_dst(int row, int lane, int j) { const int c = (lane >> 2) + 16 * j; return (row >> 4) * 16384 + (row & 15) * 16 + (c >> 2) * 1024 + (c & 3) * 256; }
DEVI bool ln_rows(const Params& p, rsrc_t rw, int r0, int lane, u64 tag) {
    const int row = r0 + (lane & 3);
    u4 v[4];
#pragma unroll
    for (int j = 0; j < 4; ++j) v[j] = *(const u4*)(p.x + (size_t)row * 1024 + ((lane >> 2) + 16 * j) * 16);
    ln_norm(v);
#pragma unroll
    for (int j = 0; j < 4; ++j) stw(v[j], rw, ln_dst(row, lane, j), WS_XN);
    if (!wait_go(p, tag, lane)) return false;
    WAIT_VM(0);
    add_flag(p, F_XN + (r0 >> 5), lane);
    return true;
}

template <int ROWB, int NU, class OFF, class TAKE> DEVI void load_panel(rsrc_t r, OFF off, lptr R, int lane, TAKE take) {
    constexpr int RPU = 8192 / ROWB;
#pragma unroll
    for (int u = 0; u < 4; ++u) stage_unit_b<ROWB>(r, off, u * RPU, R + u * 8192, lane);
#pragma unroll
    for (int u = 0; u < NU; ++u) {
        const int left = NU - 1 - u;
        if (left >= 3) WAIT_VM(24); else if (left == 2) WAIT_VM(16); else if (left == 1) WAIT_VM(8); else WAIT_VM(0);
        take(u, R + (u & 3) * 8192);
        WAIT_LGKM0();
        if (u + 4 < NU) stage_unit_b<ROWB>(r, off, (u + 4) * RPU, R + (u & 3) * 8192, lane);
    }
}

DEVI void proj_role(const Params& p, rsrc_t rw, lptr L, int kind, int pan, int s0, int ds, int w, int lane, u64 tag) {
    bf8 B[4][16];
    {
        u4 xr[8];
        const char* xs = p.x + (size_t)(s0 * 32 + w * 8) * 1024 + lane * 16;
#pragma unroll
        for (int j = 0; j < 8; ++j) asm volatile("global_load_dwordx4 %0, %1, off" : "=v"(xr[j]) : "v"(xs + j * 1024) : "memory");
        auto hook = [&]() {
            asm volatile("s_waitcnt vmcnt(32)" : "+v"(xr[0]), "+v"(xr[1]), "+v"(xr[2]), "+v"(xr[3]), "+v"(xr[4]), "+v"(xr[5]), "+v"(xr[6]), "+v"(xr[7]) :: "memory");
#pragma unroll
            for (int j = 0; j < 8; ++j) *(LAS u4*)(L + 131072 + (w >> 1) * 16384 + (lane >> 2) * 1024 + ((lane & 3) * 16 + (w & 1) * 8 + j) * 16) = ln_row64(xr[j]);
        };
        int b[4];
#pragma unroll
        for (int c = 0; c < 4; ++c) b[c] = tr_base<128>(c, lane);
        auto take = [&](int u, lptr U) {
#pragma unroll
            for (int t = 0; t < 2; ++t)
#pragma unroll
                for (int c = 0; c < 4; ++c) B[c][2 * u + t] = cat2(trd(U + b[c] + t * 4096), trd(U + b[c] + t * 4096 + 2048));
        };
        lptr R = L + w * 32768;
        const unsigned ld = kind == 2 ? FF * 2 : 8192;
        const int cs = (lane & 7) ^ (((lane >> 4) & 3) << 1);
        const char* m;
        if (kind == 0) m = (w < 2 ? p.wk : p.wv) + (pan * 128 + (w & 1) * 64) * 2 + cs * 16;
        else if (kind == 1) m = p.wq + (pan * 256 + w * 64) * 2 + cs * 16;
        else m = (cs & 4 ? p.wu : p.wg) + (pan * 128 + w * 32) * 2 + (cs & 3) * 16;
        m += (8 * (lane >> 5) + ((lane >> 3) & 3)) * ld;
        auto stage = [&](int u) {
#pragma unroll
            for (int i = 0; i < 8; ++i) glds16(m + (size_t)((64 * u + 32 * (i >> 2) + 16 * (i & 1) + 4 * ((i >> 1) & 1)) * ld), R + (u & 3) * 8192 + i * 1024);
        };
#pragma unroll
        for (int u = 0; u < 4; ++u) stage(u);
#pragma unroll
        for (int u = 0; u < 8; ++u) {
            if (u <= 4) WAIT_VM(24); else if (u == 5) WAIT_VM(16); else if (u == 6) WAIT_VM(8); else WAIT_VM(0);
            take(u, R + (u & 3) * 8192);
            WAIT_LGKM0();
            if (u < 4) stage(u + 4);
            if (u == 3) hook();
        }
    }
    LBAR();
    const bool tr = !(kind == 0 && w >= 2);
#define issueA1(S, BUF, I) __builtin_amdgcn_raw_ptr_buffer_load_lds(rw, (LAS void*)(L + 65536 + (BUF) * 32768 + (w * 8 + (I)) * 1024), 16, lane * 16, (int)((unsigned)WS_XN + (S) * 32768 + (w * 8 + (I)) * 1024), 0, 16)
    const int fl = kind == 0 ? F_KV + pan * 64 : (kind == 1 ? F_Q + pan : F_O + 1), fs = kind == 0 ? 1 : (kind == 1 ? 16 : 32);
    unsigned so;
    if (kind == 2) { so = WS_H + (pan * 4 + w) * 1024; }
    else if (tr) { so = (kind == 0 ? kx_off(0, pan) : WS_QX + (pan * 2 + (w >> 1)) * 8192) + (w & 1) * 2048; }
    else { so = kx_off(0, pan) + 8192 + (w & 1) * 4096; }
    const unsigned ss = kind == 0 ? 16384 : 32 * 8192;
    int it = 2;
    for (int s = s0; s < NSEG; s += ds, it = it == 2 ? 0 : it + 1) {
        if (s != s0) {
            if (kind == 2) WAIT_VM(2); else WAIT_VM(4);
            BAR();
        }
        const int sn = min(s + 2 * ds, NSEG - 1), bn = it == 0 ? 2 : it - 1;
        lptr A = L + 65536 + it * 32768 + lane * 16;
        f4 acc[2][4] = {};
        u4 a[3][2];
        a[0][0] = ldsr(A); a[0][1] = ldsr(A + 16384); a[1][0] = ldsr(A + 1024); a[1][1] = ldsr(A + 16384 + 1024);
#define PROJ_LOOP(MM)                                                                                                              \
        _Pragma("unroll") for (int t = 0; t < 16; ++t) {                                                                           \
            if (t + 2 < 16) { a[(t + 2) % 3][0] = ldsr(A + (t + 2) * 1024); a[(t + 2) % 3][1] = ldsr(A + 16384 + (t + 2) * 1024); } \
            if (t == 6 && s > s0 + ds) { WAIT_VM(0); add_flag(p, fl + (s - ds) * fs, lane); }     \
            if (t >= 7 && t < 15 && s != s0) issueA1(sn, bn, t - 7);                                                               \
            SB();                                                                                                                  \
            _Pragma("unroll") for (int c = 0; c < 4; ++c) { MM }                                                                   \
            SB();                                                                                                                  \
        }
        if (tr) { PROJ_LOOP(acc[0][c] = mm32(B[c][t], asbf(a[t % 3][0]), acc[0][c]); acc[1][c] = mm32(B[c][t], asbf(a[t % 3][1]), acc[1][c]);) }
        else { PROJ_LOOP(acc[0][c] = mm32(asbf(a[t % 3][0]), B[c][t], acc[0][c]); acc[1][c] = mm32(asbf(a[t % 3][1]), B[c][t], acc[1][c]);) }
        auto pack = [](f4 x, f4 y) { return (u4){pk2(x[0], x[1]), pk2(x[2], x[3]), pk2(y[0], y[1]), pk2(y[2], y[3])}; };
        if (kind == 2) {
#pragma unroll
            for (int i = 0; i < 2; ++i) {
                f4 h0, h1;
#pragma unroll
                for (int r = 0; r < 4; ++r) { h0[r] = bfr(acc[i][0][r]) * bfr(acc[i][2][r]); h1[r] = bfr(acc[i][1][r]) * bfr(acc[i][3][r]); }
                stw(pack(h0, h1), rw, lane * 16, so + s * (2 * 56 * 1024) + i * (56 * 1024));
            }
        } else if (tr) {
#pragma unroll
            for (int i = 0; i < 2; ++i)
#pragma unroll
                for (int c = 0; c < 2; ++c) stw(pack(acc[i][2 * c], acc[i][2 * c + 1]), rw, lane * 16, so + s * ss + i * 4096 + c * 1024);
        } else {
#pragma unroll
            for (int c = 0; c < 4; ++c) stw(pack(acc[0][c], acc[1][c]), rw, lane * 16, so + s * ss + c * 1024);
        }
        if (s == s0) {
            if (kind == 0 && !wait_go(p, tag, lane)) return;
            WAIT_VM(0);
            add_flag(p, fl + s0 * fs, lane);
            GAS unsigned* f = flagp(p, F_XN) + lane;
            int sp = 0;
            while (__builtin_amdgcn_ballot_w64(__hip_atomic_load(f, __ATOMIC_RELAXED, __HIP_MEMORY_SCOPE_AGENT) < 8u) != 0) { if (++sp > SPIN_CAP) return; __builtin_amdgcn_s_sleep(8); }
#pragma unroll
            for (int i = 0; i < 8; ++i) issueA1(s0 + ds, 0, i);
#pragma unroll
            for (int i = 0; i < 8; ++i) issueA1(s0 + 2 * ds, 1, i);
            WAIT_VM(8);
        }
    }
    WAIT_VM(0);
    add_flag(p, fl + (s0 + ((NSEG - 1 - s0) / ds) * ds) * fs, lane);
}

#define CHAIN_ASM \
      \
    "s_nop 4\n" \
    "s_mov_b32 s56, s36\n" \
    "s_and_b32 s57, s37, 0xffff\n" \
    "s_mov_b32 s58, 0x5e00000\n" \
    "s_mov_b32 s59, 0x20000\n" \
    "s_lshl_b32 s75, s47, 15\n" \
    "s_add_u32 s75, s75, s50\n" \
    "s_lshl_b32 s74, s47, 19\n" \
    "s_add_u32 s62, s36, 3178496\n" \
    "s_addc_u32 s63, s37, 0\n" \
    "v_mov_b32 v19, 0\n" \
    "global_load_dwordx2 v[124:125], v19, s[62:63] sc1\n" \
    "s_mov_b32 s52, s38\n" \
    "s_and_b32 s53, s39, 0xffff\n" \
    "s_mov_b32 s54, 0x400000\n" \
    "s_mov_b32 s55, 0x20000\n" \
    "s_add_u32 m0, s75, 0\n" \
    "s_add_u32 s77, s74, 0\n" \
    "buffer_load_dwordx4 v28, s[52:55], s77 offen lds\n" \
    "s_add_u32 m0, s75, 1024\n" \
    "s_add_u32 s77, s74, 16384\n" \
    "buffer_load_dwordx4 v29, s[52:55], s77 offen lds\n" \
    "s_add_u32 m0, s75, 2048\n" \
    "s_add_u32 s77, s74, 32768\n" \
    "buffer_load_dwordx4 v28, s[52:55], s77 offen lds\n" \
    "s_add_u32 m0, s75, 3072\n" \
    "s_add_u32 s77, s74, 49152\n" \
    "buffer_load_dwordx4 v29, s[52:55], s77 offen lds\n" \
    "s_add_u32 m0, s75, 4096\n" \
    "s_add_u32 s77, s74, 262144\n" \
    "buffer_load_dwordx4 v28, s[52:55], s77 offen lds\n" \
    "s_add_u32 m0, s75, 5120\n" \
    "s_add_u32 s77, s74, 278528\n" \
    "buffer_load_dwordx4 v29, s[52:55], s77 offen lds\n" \
    "s_add_u32 m0, s75, 6144\n" \
    "s_add_u32 s77, s74, 294912\n" \
    "buffer_load_dwordx4 v28, s[52:55], s77 offen lds\n" \
    "s_add_u32 m0, s75, 7168\n" \
    "s_add_u32 s77, s74, 311296\n" \
    "buffer_load_dwordx4 v29, s[52:55], s77 offen lds\n" \
    "s_add_u32 m0, s75, 8192\n" \
    "s_add_u32 s77, s74, 65536\n" \
    "buffer_load_dwordx4 v28, s[52:55], s77 offen lds\n" \
    "s_add_u32 m0, s75, 9216\n" \
    "s_add_u32 s77, s74, 81920\n" \
    "buffer_load_dwordx4 v29, s[52:55], s77 offen lds\n" \
    "s_add_u32 m0, s75, 10240\n" \
    "s_add_u32 s77, s74, 98304\n" \
    "buffer_load_dwordx4 v28, s[52:55], s77 offen lds\n" \
    "s_add_u32 m0, s75, 11264\n" \
    "s_add_u32 s77, s74, 114688\n" \
    "buffer_load_dwordx4 v29, s[52:55], s77 offen lds\n" \
    "s_add_u32 m0, s75, 12288\n" \
    "s_add_u32 s77, s74, 327680\n" \
    "buffer_load_dwordx4 v28, s[52:55], s77 offen lds\n" \
    "s_add_u32 m0, s75, 13312\n" \
    "s_add_u32 s77, s74, 344064\n" \
    "buffer_load_dwordx4 v29, s[52:55], s77 offen lds\n" \
    "s_add_u32 m0, s75, 14336\n" \
    "s_add_u32 s77, s74, 360448\n" \
    "buffer_load_dwordx4 v28, s[52:55], s77 offen lds\n" \
    "s_add_u32 m0, s75, 15360\n" \
    "s_add_u32 s77, s74, 376832\n" \
    "buffer_load_dwordx4 v29, s[52:55], s77 offen lds\n" \
    "s_add_u32 m0, s75, 16384\n" \
    "s_add_u32 s77, s74, 2097152\n" \
    "buffer_load_dwordx4 v28, s[52:55], s77 offen lds\n" \
    "s_add_u32 m0, s75, 17408\n" \
    "s_add_u32 s77, s74, 2113536\n" \
    "buffer_load_dwordx4 v29, s[52:55], s77 offen lds\n" \
    "s_add_u32 m0, s75, 18432\n" \
    "s_add_u32 s77, s74, 2129920\n" \
    "buffer_load_dwordx4 v28, s[52:55], s77 offen lds\n" \
    "s_add_u32 m0, s75, 19456\n" \
    "s_add_u32 s77, s74, 2146304\n" \
    "buffer_load_dwordx4 v29, s[52:55], s77 offen lds\n" \
    "s_add_u32 m0, s75, 20480\n" \
    "s_add_u32 s77, s74, 2359296\n" \
    "buffer_load_dwordx4 v28, s[52:55], s77 offen lds\n" \
    "s_add_u32 m0, s75, 21504\n" \
    "s_add_u32 s77, s74, 2375680\n" \
    "buffer_load_dwordx4 v29, s[52:55], s77 offen lds\n" \
    "s_add_u32 m0, s75, 22528\n" \
    "s_add_u32 s77, s74, 2392064\n" \
    "buffer_load_dwordx4 v28, s[52:55], s77 offen lds\n" \
    "s_add_u32 m0, s75, 23552\n" \
    "s_add_u32 s77, s74, 2408448\n" \
    "buffer_load_dwordx4 v29, s[52:55], s77 offen lds\n" \
    "s_add_u32 m0, s75, 24576\n" \
    "s_add_u32 s77, s74, 2162688\n" \
    "buffer_load_dwordx4 v28, s[52:55], s77 offen lds\n" \
    "s_add_u32 m0, s75, 25600\n" \
    "s_add_u32 s77, s74, 2179072\n" \
    "buffer_load_dwordx4 v29, s[52:55], s77 offen lds\n" \
    "s_add_u32 m0, s75, 26624\n" \
    "s_add_u32 s77, s74, 2195456\n" \
    "buffer_load_dwordx4 v28, s[52:55], s77 offen lds\n" \
    "s_add_u32 m0, s75, 27648\n" \
    "s_add_u32 s77, s74, 2211840\n" \
    "buffer_load_dwordx4 v29, s[52:55], s77 offen lds\n" \
    "s_add_u32 m0, s75, 28672\n" \
    "s_add_u32 s77, s74, 2424832\n" \
    "buffer_load_dwordx4 v28, s[52:55], s77 offen lds\n" \
    "s_add_u32 m0, s75, 29696\n" \
    "s_add_u32 s77, s74, 2441216\n" \
    "buffer_load_dwordx4 v29, s[52:55], s77 offen lds\n" \
    "s_add_u32 m0, s75, 30720\n" \
    "s_add_u32 s77, s74, 2457600\n" \
    "buffer_load_dwordx4 v28, s[52:55], s77 offen lds\n" \
    "s_add_u32 m0, s75, 31744\n" \
    "s_add_u32 s77, s74, 2473984\n" \
    "buffer_load_dwordx4 v29, s[52:55], s77 offen lds\n" \
    "s_mov_b32 s64, s48\n" \
    "s_and_b32 s65, s49, 0xffff\n" \
    "s_mov_b32 s66, 8192\n" \
    "s_mov_b32 s67, 0x20000\n" \
    "s_lshl_b32 s76, s47, 11\n" \
    "s_add_u32 s77, s76, 131072\n" \
    "s_add_u32 m0, s77, s50\n" \
    "s_nop 0\n" \
    "buffer_load_dwordx4 v0, s[64:67], s76 offen lds\n" \
    "buffer_load_dwordx4 v0, s[64:67], s76 offen offset:1024 lds\n" \
    "s_waitcnt vmcnt(18)\n" \
    "s_barrier\n" \
    "v_readfirstlane_b32 s96, v124\n" \
    "v_readfirstlane_b32 s97, v125\n" \
    "s_nop 1\n" \
    "s_add_u32 s96, s96, 1\n" \
    "s_addc_u32 s97, s97, 0\n" \
    "s_lshl_b64 s[96:97], s[96:97], 3\n" \
    "s_xor_b32 s76, s47, 0\n" \
    "s_lshl_b32 s76, s76, 15\n" \
    "v_add_u32 v34, s76, v32\n" \
    "v_xor_b32 v35, 32, v34\n" \
    "ds_read_b64_tr_b16 v[36:37], v34\n" \
    "ds_read_b64_tr_b16 v[38:39], v34 offset:4096\n" \
    "ds_read_b64_tr_b16 v[40:41], v34 offset:8192\n" \
    "ds_read_b64_tr_b16 v[42:43], v34 offset:12288\n" \
    "ds_read_b64_tr_b16 v[44:45], v35\n" \
    "ds_read_b64_tr_b16 v[46:47], v35 offset:4096\n" \
    "ds_read_b64_tr_b16 v[48:49], v35 offset:8192\n" \
    "ds_read_b64_tr_b16 v[50:51], v35 offset:12288\n" \
    "s_xor_b32 s76, s47, 1\n" \
    "s_lshl_b32 s76, s76, 15\n" \
    "v_add_u32 v34, s76, v32\n" \
    "v_xor_b32 v35, 32, v34\n" \
    "ds_read_b64_tr_b16 a[16:17], v34\n" \
    "ds_read_b64_tr_b16 a[18:19], v34 offset:4096\n" \
    "ds_read_b64_tr_b16 a[20:21], v34 offset:8192\n" \
    "ds_read_b64_tr_b16 a[22:23], v34 offset:12288\n" \
    "ds_read_b64_tr_b16 a[80:81], v35\n" \
    "ds_read_b64_tr_b16 a[82:83], v35 offset:4096\n" \
    "ds_read_b64_tr_b16 a[84:85], v35 offset:8192\n" \
    "s_waitcnt lgkmcnt(14)\n" \
    "ds_read_b64_tr_b16 a[86:87], v35 offset:12288\n" \
    "s_xor_b32 s76, s47, 2\n" \
    "s_lshl_b32 s76, s76, 15\n" \
    "v_add_u32 v34, s76, v32\n" \
    "v_xor_b32 v35, 32, v34\n" \
    "s_waitcnt lgkmcnt(14)\n" \
    "ds_read_b64_tr_b16 a[32:33], v34\n" \
    "s_waitcnt lgkmcnt(14)\n" \
    "ds_read_b64_tr_b16 a[34:35], v34 offset:4096\n" \
    "s_waitcnt lgkmcnt(14)\n" \
    "ds_read_b64_tr_b16 a[36:37], v34 offset:8192\n" \
    "s_waitcnt lgkmcnt(14)\n" \
    "ds_read_b64_tr_b16 a[38:39], v34 offset:12288\n" \
    "s_waitcnt lgkmcnt(14)\n" \
    "ds_read_b64_tr_b16 a[96:97], v35\n" \
    "s_waitcnt lgkmcnt(14)\n" \
    "ds_read_b64_tr_b16 a[98:99], v35 offset:4096\n" \
    "s_waitcnt lgkmcnt(14)\n" \
    "ds_read_b64_tr_b16 a[100:101], v35 offset:8192\n" \
    "s_waitcnt lgkmcnt(14)\n" \
    "ds_read_b64_tr_b16 a[102:103], v35 offset:12288\n" \
    "s_xor_b32 s76, s47, 3\n" \
    "s_lshl_b32 s76, s76, 15\n" \
    "v_add_u32 v34, s76, v32\n" \
    "v_xor_b32 v35, 32, v34\n" \
    "s_waitcnt lgkmcnt(14)\n" \
    "ds_read_b64_tr_b16 a[48:49], v34\n" \
    "s_waitcnt lgkmcnt(14)\n" \
    "ds_read_b64_tr_b16 a[50:51], v34 offset:4096\n" \
    "s_waitcnt lgkmcnt(14)\n" \
    "ds_read_b64_tr_b16 a[52:53], v34 offset:8192\n" \
    "s_waitcnt lgkmcnt(14)\n" \
    "ds_read_b64_tr_b16 a[54:55], v34 offset:12288\n" \
    "s_waitcnt lgkmcnt(14)\n" \
    "ds_read_b64_tr_b16 a[112:113], v35\n" \
    "s_waitcnt lgkmcnt(14)\n" \
    "ds_read_b64_tr_b16 a[114:115], v35 offset:4096\n" \
    "s_waitcnt lgkmcnt(14)\n" \
    "ds_read_b64_tr_b16 a[116:117], v35 offset:8192\n" \
    "s_waitcnt lgkmcnt(14)\n" \
    "ds_read_b64_tr_b16 a[118:119], v35 offset:12288\n" \
    "s_waitcnt lgkmcnt(0)\n" \
    "s_barrier\n" \
    "s_mov_b32 s52, s40\n" \
    "s_and_b32 s53, s41, 0xffff\n" \
    "s_mov_b32 s54, 0x400000\n" \
    "s_mov_b32 s55, 0x20000\n" \
    "s_add_u32 m0, s75, 0\n" \
    "s_add_u32 s77, s74, 0\n" \
    "buffer_load_dwordx4 v28, s[52:55], s77 offen lds\n" \
    "s_add_u32 m0, s75, 1024\n" \
    "s_add_u32 s77, s74, 16384\n" \
    "buffer_load_dwordx4 v29, s[52:55], s77 offen lds\n" \
    "s_add_u32 m0, s75, 2048\n" \
    "s_add_u32 s77, s74, 32768\n" \
    "buffer_load_dwordx4 v28, s[52:55], s77 offen lds\n" \
    "s_add_u32 m0, s75, 3072\n" \
    "s_add_u32 s77, s74, 49152\n" \
    "buffer_load_dwordx4 v29, s[52:55], s77 offen lds\n" \
    "s_add_u32 m0, s75, 4096\n" \
    "s_add_u32 s77, s74, 262144\n" \
    "buffer_load_dwordx4 v28, s[52:55], s77 offen lds\n" \
    "s_add_u32 m0, s75, 5120\n" \
    "s_add_u32 s77, s74, 278528\n" \
    "buffer_load_dwordx4 v29, s[52:55], s77 offen lds\n" \
    "s_add_u32 m0, s75, 6144\n" \
    "s_add_u32 s77, s74, 294912\n" \
    "buffer_load_dwordx4 v28, s[52:55], s77 offen lds\n" \
    "s_add_u32 m0, s75, 7168\n" \
    "s_add_u32 s77, s74, 311296\n" \
    "buffer_load_dwordx4 v29, s[52:55], s77 offen lds\n" \
    "s_add_u32 m0, s75, 8192\n" \
    "s_add_u32 s77, s74, 65536\n" \
    "buffer_load_dwordx4 v28, s[52:55], s77 offen lds\n" \
    "s_add_u32 m0, s75, 9216\n" \
    "s_add_u32 s77, s74, 81920\n" \
    "buffer_load_dwordx4 v29, s[52:55], s77 offen lds\n" \
    "s_add_u32 m0, s75, 10240\n" \
    "s_add_u32 s77, s74, 98304\n" \
    "buffer_load_dwordx4 v28, s[52:55], s77 offen lds\n" \
    "s_add_u32 m0, s75, 11264\n" \
    "s_add_u32 s77, s74, 114688\n" \
    "buffer_load_dwordx4 v29, s[52:55], s77 offen lds\n" \
    "s_add_u32 m0, s75, 12288\n" \
    "s_add_u32 s77, s74, 327680\n" \
    "buffer_load_dwordx4 v28, s[52:55], s77 offen lds\n" \
    "s_add_u32 m0, s75, 13312\n" \
    "s_add_u32 s77, s74, 344064\n" \
    "buffer_load_dwordx4 v29, s[52:55], s77 offen lds\n" \
    "s_add_u32 m0, s75, 14336\n" \
    "s_add_u32 s77, s74, 360448\n" \
    "buffer_load_dwordx4 v28, s[52:55], s77 offen lds\n" \
    "s_add_u32 m0, s75, 15360\n" \
    "s_add_u32 s77, s74, 376832\n" \
    "buffer_load_dwordx4 v29, s[52:55], s77 offen lds\n" \
    "s_waitcnt vmcnt(18)\n" \
    "s_barrier\n" \
    "s_xor_b32 s76, s47, 0\n" \
    "s_lshl_b32 s76, s76, 15\n" \
    "v_add_u32 v34, s76, v32\n" \
    "v_xor_b32 v35, 32, v34\n" \
    "ds_read_b64_tr_b16 v[52:53], v34 offset:16384\n" \
    "ds_read_b64_tr_b16 v[54:55], v34 offset:20480\n" \
    "ds_read_b64_tr_b16 v[56:57], v34 offset:24576\n" \
    "ds_read_b64_tr_b16 v[58:59], v34 offset:28672\n" \
    "ds_read_b64_tr_b16 v[60:61], v35 offset:16384\n" \
    "ds_read_b64_tr_b16 v[62:63], v35 offset:20480\n" \
    "ds_read_b64_tr_b16 v[64:65], v35 offset:24576\n" \
    "ds_read_b64_tr_b16 v[66:67], v35 offset:28672\n" \
    "s_xor_b32 s76, s47, 1\n" \
    "s_lshl_b32 s76, s76, 15\n" \
    "v_add_u32 v34, s76, v32\n" \
    "v_xor_b32 v35, 32, v34\n" \
    "ds_read_b64_tr_b16 a[24:25], v34 offset:16384\n" \
    "ds_read_b64_tr_b16 a[26:27], v34 offset:20480\n" \
    "ds_read_b64_tr_b16 a[28:29], v34 offset:24576\n" \
    "ds_read_b64_tr_b16 a[30:31], v34 offset:28672\n" \
    "ds_read_b64_tr_b16 a[88:89], v35 offset:16384\n" \
    "ds_read_b64_tr_b16 a[90:91], v35 offset:20480\n" \
    "ds_read_b64_tr_b16 a[92:93], v35 offset:24576\n" \
    "s_waitcnt lgkmcnt(14)\n" \
    "ds_read_b64_tr_b16 a[94:95], v35 offset:28672\n" \
    "s_xor_b32 s76, s47, 2\n" \
    "s_lshl_b32 s76, s76, 15\n" \
    "v_add_u32 v34, s76, v32\n" \
    "v_xor_b32 v35, 32, v34\n" \
    "s_waitcnt lgkmcnt(14)\n" \
    "ds_read_b64_tr_b16 a[40:41], v34 offset:16384\n" \
    "s_waitcnt lgkmcnt(14)\n" \
    "ds_read_b64_tr_b16 a[42:43], v34 offset:20480\n" \
    "s_waitcnt lgkmcnt(14)\n" \
    "ds_read_b64_tr_b16 a[44:45], v34 offset:24576\n" \
    "s_waitcnt lgkmcnt(14)\n" \
    "ds_read_b64_tr_b16 a[46:47], v34 offset:28672\n" \
    "s_waitcnt lgkmcnt(14)\n" \
    "ds_read_b64_tr_b16 a[104:105], v35 offset:16384\n" \
    "s_waitcnt lgkmcnt(14)\n" \
    "ds_read_b64_tr_b16 a[106:107], v35 offset:20480\n" \
    "s_waitcnt lgkmcnt(14)\n" \
    "ds_read_b64_tr_b16 a[108:109], v35 offset:24576\n" \
    "s_waitcnt lgkmcnt(14)\n" \
    "ds_read_b64_tr_b16 a[110:111], v35 offset:28672\n" \
    "s_xor_b32 s76, s47, 3\n" \
    "s_lshl_b32 s76, s76, 15\n" \
    "v_add_u32 v34, s76, v32\n" \
    "v_xor_b32 v35, 32, v34\n" \
    "s_waitcnt lgkmcnt(14)\n" \
    "ds_read_b64_tr_b16 a[56:57], v34 offset:16384\n" \
    "s_waitcnt lgkmcnt(14)\n" \
    "ds_read_b64_tr_b16 a[58:59], v34 offset:20480\n" \
    "s_waitcnt lgkmcnt(14)\n" \
    "ds_read_b64_tr_b16 a[60:61], v34 offset:24576\n" \
    "s_waitcnt lgkmcnt(14)\n" \
    "ds_read_b64_tr_b16 a[62:63], v34 offset:28672\n" \
    "s_waitcnt lgkmcnt(14)\n" \
    "ds_read_b64_tr_b16 a[120:121], v35 offset:16384\n" \
    "s_waitcnt lgkmcnt(14)\n" \
    "ds_read_b64_tr_b16 a[122:123], v35 offset:20480\n" \
    "s_waitcnt lgkmcnt(14)\n" \
    "ds_read_b64_tr_b16 a[124:125], v35 offset:24576\n" \
    "s_waitcnt lgkmcnt(14)\n" \
    "ds_read_b64_tr_b16 a[126:127], v35 offset:28672\n" \
    "s_waitcnt lgkmcnt(0)\n" \
    "s_barrier\n" \
    "s_add_u32 m0, s75, 16384\n" \
    "s_add_u32 s77, s74, 2097152\n" \
    "buffer_load_dwordx4 v28, s[52:55], s77 offen lds\n" \
    "s_add_u32 m0, s75, 17408\n" \
    "s_add_u32 s77, s74, 2113536\n" \
    "buffer_load_dwordx4 v29, s[52:55], s77 offen lds\n" \
    "s_add_u32 m0, s75, 18432\n" \
    "s_add_u32 s77, s74, 2129920\n" \
    "buffer_load_dwordx4 v28, s[52:55], s77 offen lds\n" \
    "s_add_u32 m0, s75, 19456\n" \
    "s_add_u32 s77, s74, 2146304\n" \
    "buffer_load_dwordx4 v29, s[52:55], s77 offen lds\n" \
    "s_add_u32 m0, s75, 20480\n" \
    "s_add_u32 s77, s74, 2359296\n" \
    "buffer_load_dwordx4 v28, s[52:55], s77 offen lds\n" \
    "s_add_u32 m0, s75, 21504\n" \
    "s_add_u32 s77, s74, 2375680\n" \
    "buffer_load_dwordx4 v29, s[52:55], s77 offen lds\n" \
    "s_add_u32 m0, s75, 22528\n" \
    "s_add_u32 s77, s74, 2392064\n" \
    "buffer_load_dwordx4 v28, s[52:55], s77 offen lds\n" \
    "s_add_u32 m0, s75, 23552\n" \
    "s_add_u32 s77, s74, 2408448\n" \
    "buffer_load_dwordx4 v29, s[52:55], s77 offen lds\n" \
    "s_add_u32 m0, s75, 24576\n" \
    "s_add_u32 s77, s74, 2162688\n" \
    "buffer_load_dwordx4 v28, s[52:55], s77 offen lds\n" \
    "s_add_u32 m0, s75, 25600\n" \
    "s_add_u32 s77, s74, 2179072\n" \
    "buffer_load_dwordx4 v29, s[52:55], s77 offen lds\n" \
    "s_add_u32 m0, s75, 26624\n" \
    "s_add_u32 s77, s74, 2195456\n" \
    "buffer_load_dwordx4 v28, s[52:55], s77 offen lds\n" \
    "s_add_u32 m0, s75, 27648\n" \
    "s_add_u32 s77, s74, 2211840\n" \
    "buffer_load_dwordx4 v29, s[52:55], s77 offen lds\n" \
    "s_add_u32 m0, s75, 28672\n" \
    "s_add_u32 s77, s74, 2424832\n" \
    "buffer_load_dwordx4 v28, s[52:55], s77 offen lds\n" \
    "s_add_u32 m0, s75, 29696\n" \
    "s_add_u32 s77, s74, 2441216\n" \
    "buffer_load_dwordx4 v29, s[52:55], s77 offen lds\n" \
    "s_add_u32 m0, s75, 30720\n" \
    "s_add_u32 s77, s74, 2457600\n" \
    "buffer_load_dwordx4 v28, s[52:55], s77 offen lds\n" \
    "s_add_u32 m0, s75, 31744\n" \
    "s_add_u32 s77, s74, 2473984\n" \
    "buffer_load_dwordx4 v29, s[52:55], s77 offen lds\n" \
    "s_waitcnt vmcnt(16)\n" \
    "s_barrier\n" \
    "ds_read_u16 v20, v17 offset:0\n" \
    "ds_read_u16 v116, v17 offset:32\n" \
    "ds_read_u16 v21, v17 offset:64\n" \
    "ds_read_u16 v117, v17 offset:96\n" \
    "ds_read_u16 v22, v17 offset:4\n" \
    "ds_read_u16 v118, v17 offset:36\n" \
    "ds_read_u16 v23, v17 offset:68\n" \
    "ds_read_u16 v119, v17 offset:100\n" \
    "ds_read_u16 v24, v17 offset:512\n" \
    "ds_read_u16 v120, v17 offset:544\n" \
    "ds_read_u16 v25, v17 offset:576\n" \
    "ds_read_u16 v121, v17 offset:608\n" \
    "ds_read_u16 v26, v17 offset:516\n" \
    "ds_read_u16 v122, v17 offset:548\n" \
    "ds_read_u16 v27, v17 offset:580\n" \
    "s_waitcnt lgkmcnt(14)\n" \
    "ds_read_u16 v123, v17 offset:612\n" \
    "s_waitcnt lgkmcnt(0)\n" \
    "v_lshl_or_b32 v20, v116, 16, v20\n" \
    "v_lshl_or_b32 v21, v117, 16, v21\n" \
    "v_lshl_or_b32 v22, v118, 16, v22\n" \
    "v_lshl_or_b32 v23, v119, 16, v23\n" \
    "v_lshl_or_b32 v24, v120, 16, v24\n" \
    "v_lshl_or_b32 v25, v121, 16, v25\n" \
    "v_lshl_or_b32 v26, v122, 16, v26\n" \
    "v_lshl_or_b32 v27, v123, 16, v27\n" \
    "s_xor_b32 s76, s47, 0\n" \
    "s_lshl_b32 s76, s76, 15\n" \
    "v_add_u32 v34, s76, v32\n" \
    "v_xor_b32 v35, 32, v34\n" \
    "ds_read_b64_tr_b16 v[68:69], v34\n" \
    "ds_read_b64_tr_b16 v[70:71], v34 offset:4096\n" \
    "ds_read_b64_tr_b16 v[72:73], v34 offset:8192\n" \
    "ds_read_b64_tr_b16 v[74:75], v34 offset:12288\n" \
    "ds_read_b64_tr_b16 v[76:77], v35\n" \
    "ds_read_b64_tr_b16 v[78:79], v35 offset:4096\n" \
    "ds_read_b64_tr_b16 v[80:81], v35 offset:8192\n" \
    "ds_read_b64_tr_b16 v[82:83], v35 offset:12288\n" \
    "s_xor_b32 s76, s47, 1\n" \
    "s_lshl_b32 s76, s76, 15\n" \
    "v_add_u32 v34, s76, v32\n" \
    "v_xor_b32 v35, 32, v34\n" \
    "ds_read_b64_tr_b16 a[144:145], v34\n" \
    "ds_read_b64_tr_b16 a[146:147], v34 offset:4096\n" \
    "ds_read_b64_tr_b16 a[148:149], v34 offset:8192\n" \
    "ds_read_b64_tr_b16 a[150:151], v34 offset:12288\n" \
    "ds_read_b64_tr_b16 a[208:209], v35\n" \
    "ds_read_b64_tr_b16 a[210:211], v35 offset:4096\n" \
    "ds_read_b64_tr_b16 a[212:213], v35 offset:8192\n" \
    "s_waitcnt lgkmcnt(14)\n" \
    "ds_read_b64_tr_b16 a[214:215], v35 offset:12288\n" \
    "s_xor_b32 s76, s47, 2\n" \
    "s_lshl_b32 s76, s76, 15\n" \
    "v_add_u32 v34, s76, v32\n" \
    "v_xor_b32 v35, 32, v34\n" \
    "s_waitcnt lgkmcnt(14)\n" \
    "ds_read_b64_tr_b16 a[160:161], v34\n" \
    "s_waitcnt lgkmcnt(14)\n" \
    "ds_read_b64_tr_b16 a[162:163], v34 offset:4096\n" \
    "s_waitcnt lgkmcnt(14)\n" \
    "ds_read_b64_tr_b16 a[164:165], v34 offset:8192\n" \
    "s_waitcnt lgkmcnt(14)\n" \
    "ds_read_b64_tr_b16 a[166:167], v34 offset:12288\n" \
    "s_waitcnt lgkmcnt(14)\n" \
    "ds_read_b64_tr_b16 a[224:225], v35\n" \
    "s_waitcnt lgkmcnt(14)\n" \
    "ds_read_b64_tr_b16 a[226:227], v35 offset:4096\n" \
    "s_waitcnt lgkmcnt(14)\n" \
    "ds_read_b64_tr_b16 a[228:229], v35 offset:8192\n" \
    "s_waitcnt lgkmcnt(14)\n" \
    "ds_read_b64_tr_b16 a[230:231], v35 offset:12288\n" \
    "s_xor_b32 s76, s47, 3\n" \
    "s_lshl_b32 s76, s76, 15\n" \
    "v_add_u32 v34, s76, v32\n" \
    "v_xor_b32 v35, 32, v34\n" \
    "s_waitcnt lgkmcnt(14)\n" \
    "ds_read_b64_tr_b16 a[176:177], v34\n" \
    "s_waitcnt lgkmcnt(14)\n" \
    "ds_read_b64_tr_b16 a[178:179], v34 offset:4096\n" \
    "s_waitcnt lgkmcnt(14)\n" \
    "ds_read_b64_tr_b16 a[180:181], v34 offset:8192\n" \
    "s_waitcnt lgkmcnt(14)\n" \
    "ds_read_b64_tr_b16 a[182:183], v34 offset:12288\n" \
    "s_waitcnt lgkmcnt(14)\n" \
    "ds_read_b64_tr_b16 a[240:241], v35\n" \
    "s_waitcnt lgkmcnt(14)\n" \
    "ds_read_b64_tr_b16 a[242:243], v35 offset:4096\n" \
    "s_waitcnt lgkmcnt(14)\n" \
    "ds_read_b64_tr_b16 a[244:245], v35 offset:8192\n" \
    "s_waitcnt lgkmcnt(14)\n" \
    "ds_read_b64_tr_b16 a[246:247], v35 offset:12288\n" \
    "s_waitcnt lgkmcnt(0)\n" \
    "s_barrier\n" \
    "s_mov_b32 s52, s42\n" \
    "s_and_b32 s53, s43, 0xffff\n" \
    "s_mov_b32 s54, 0x400000\n" \
    "s_mov_b32 s55, 0x20000\n" \
    "s_add_u32 m0, s75, 0\n" \
    "s_add_u32 s77, s74, 0\n" \
    "buffer_load_dwordx4 v28, s[52:55], s77 offen lds\n" \
    "s_add_u32 m0, s75, 1024\n" \
    "s_add_u32 s77, s74, 16384\n" \
    "buffer_load_dwordx4 v29, s[52:55], s77 offen lds\n" \
    "s_add_u32 m0, s75, 2048\n" \
    "s_add_u32 s77, s74, 32768\n" \
    "buffer_load_dwordx4 v28, s[52:55], s77 offen lds\n" \
    "s_add_u32 m0, s75, 3072\n" \
    "s_add_u32 s77, s74, 49152\n" \
    "buffer_load_dwordx4 v29, s[52:55], s77 offen lds\n" \
    "s_add_u32 m0, s75, 4096\n" \
    "s_add_u32 s77, s74, 262144\n" \
    "buffer_load_dwordx4 v28, s[52:55], s77 offen lds\n" \
    "s_add_u32 m0, s75, 5120\n" \
    "s_add_u32 s77, s74, 278528\n" \
    "buffer_load_dwordx4 v29, s[52:55], s77 offen lds\n" \
    "s_add_u32 m0, s75, 6144\n" \
    "s_add_u32 s77, s74, 294912\n" \
    "buffer_load_dwordx4 v28, s[52:55], s77 offen lds\n" \
    "s_add_u32 m0, s75, 7168\n" \
    "s_add_u32 s77, s74, 311296\n" \
    "buffer_load_dwordx4 v29, s[52:55], s77 offen lds\n" \
    "s_add_u32 m0, s75, 8192\n" \
    "s_add_u32 s77, s74, 65536\n" \
    "buffer_load_dwordx4 v28, s[52:55], s77 offen lds\n" \
    "s_add_u32 m0, s75, 9216\n" \
    "s_add_u32 s77, s74, 81920\n" \
    "buffer_load_dwordx4 v29, s[52:55], s77 offen lds\n" \
    "s_add_u32 m0, s75, 10240\n" \
    "s_add_u32 s77, s74, 98304\n" \
    "buffer_load_dwordx4 v28, s[52:55], s77 offen lds\n" \
    "s_add_u32 m0, s75, 11264\n" \
    "s_add_u32 s77, s74, 114688\n" \
    "buffer_load_dwordx4 v29, s[52:55], s77 offen lds\n" \
    "s_add_u32 m0, s75, 12288\n" \
    "s_add_u32 s77, s74, 327680\n" \
    "buffer_load_dwordx4 v28, s[52:55], s77 offen lds\n" \
    "s_add_u32 m0, s75, 13312\n" \
    "s_add_u32 s77, s74, 344064\n" \
    "buffer_load_dwordx4 v29, s[52:55], s77 offen lds\n" \
    "s_add_u32 m0, s75, 14336\n" \
    "s_add_u32 s77, s74, 360448\n" \
    "buffer_load_dwordx4 v28, s[52:55], s77 offen lds\n" \
    "s_add_u32 m0, s75, 15360\n" \
    "s_add_u32 s77, s74, 376832\n" \
    "buffer_load_dwordx4 v29, s[52:55], s77 offen lds\n" \
    "s_waitcnt vmcnt(16)\n" \
    "s_barrier\n" \
    "s_xor_b32 s76, s47, 0\n" \
    "s_lshl_b32 s76, s76, 15\n" \
    "v_add_u32 v34, s76, v32\n" \
    "v_xor_b32 v35, 32, v34\n" \
    "ds_read_b64_tr_b16 v[84:85], v34 offset:16384\n" \
    "ds_read_b64_tr_b16 v[86:87], v34 offset:20480\n" \
    "ds_read_b64_tr_b16 v[88:89], v34 offset:24576\n" \
    "ds_read_b64_tr_b16 v[90:91], v34 offset:28672\n" \
    "ds_read_b64_tr_b16 v[92:93], v35 offset:16384\n" \
    "ds_read_b64_tr_b16 v[94:95], v35 offset:20480\n" \
    "ds_read_b64_tr_b16 v[96:97], v35 offset:24576\n" \
    "ds_read_b64_tr_b16 v[98:99], v35 offset:28672\n" \
    "s_xor_b32 s76, s47, 1\n" \
    "s_lshl_b32 s76, s76, 15\n" \
    "v_add_u32 v34, s76, v32\n" \
    "v_xor_b32 v35, 32, v34\n" \
    "ds_read_b64_tr_b16 a[152:153], v34 offset:16384\n" \
    "ds_read_b64_tr_b16 a[154:155], v34 offset:20480\n" \
    "ds_read_b64_tr_b16 a[156:157], v34 offset:24576\n" \
    "ds_read_b64_tr_b16 a[158:159], v34 offset:28672\n" \
    "ds_read_b64_tr_b16 a[216:217], v35 offset:16384\n" \
    "ds_read_b64_tr_b16 a[218:219], v35 offset:20480\n" \
    "ds_read_b64_tr_b16 a[220:221], v35 offset:24576\n" \
    "s_waitcnt lgkmcnt(14)\n" \
    "ds_read_b64_tr_b16 a[222:223], v35 offset:28672\n" \
    "s_xor_b32 s76, s47, 2\n" \
    "s_lshl_b32 s76, s76, 15\n" \
    "v_add_u32 v34, s76, v32\n" \
    "v_xor_b32 v35, 32, v34\n" \
    "s_waitcnt lgkmcnt(14)\n" \
    "ds_read_b64_tr_b16 a[168:169], v34 offset:16384\n" \
    "s_waitcnt lgkmcnt(14)\n" \
    "ds_read_b64_tr_b16 a[170:171], v34 offset:20480\n" \
    "s_waitcnt lgkmcnt(14)\n" \
    "ds_read_b64_tr_b16 a[172:173], v34 offset:24576\n" \
    "s_waitcnt lgkmcnt(14)\n" \
    "ds_read_b64_tr_b16 a[174:175], v34 offset:28672\n" \
    "s_waitcnt lgkmcnt(14)\n" \
    "ds_read_b64_tr_b16 a[232:233], v35 offset:16384\n" \
    "s_waitcnt lgkmcnt(14)\n" \
    "ds_read_b64_tr_b16 a[234:235], v35 offset:20480\n" \
    "s_waitcnt lgkmcnt(14)\n" \
    "ds_read_b64_tr_b16 a[236:237], v35 offset:24576\n" \
    "s_waitcnt lgkmcnt(14)\n" \
    "ds_read_b64_tr_b16 a[238:239], v35 offset:28672\n" \
    "s_xor_b32 s76, s47, 3\n" \
    "s_lshl_b32 s76, s76, 15\n" \
    "v_add_u32 v34, s76, v32\n" \
    "v_xor_b32 v35, 32, v34\n" \
    "s_waitcnt lgkmcnt(14)\n" \
    "ds_read_b64_tr_b16 a[184:185], v34 offset:16384\n" \
    "s_waitcnt lgkmcnt(14)\n" \
    "ds_read_b64_tr_b16 a[186:187], v34 offset:20480\n" \
    "s_waitcnt lgkmcnt(14)\n" \
    "ds_read_b64_tr_b16 a[188:189], v34 offset:24576\n" \
    "s_waitcnt lgkmcnt(14)\n" \
    "ds_read_b64_tr_b16 a[190:191], v34 offset:28672\n" \
    "s_waitcnt lgkmcnt(14)\n" \
    "ds_read_b64_tr_b16 a[248:249], v35 offset:16384\n" \
    "s_waitcnt lgkmcnt(14)\n" \
    "ds_read_b64_tr_b16 a[250:251], v35 offset:20480\n" \
    "s_waitcnt lgkmcnt(14)\n" \
    "ds_read_b64_tr_b16 a[252:253], v35 offset:24576\n" \
    "s_waitcnt lgkmcnt(14)\n" \
    "ds_read_b64_tr_b16 a[254:255], v35 offset:28672\n" \
    "s_waitcnt lgkmcnt(0)\n" \
    "s_barrier\n" \
    "s_add_u32 m0, s75, 16384\n" \
    "s_add_u32 s77, s74, 2097152\n" \
    "buffer_load_dwordx4 v28, s[52:55], s77 offen lds\n" \
    "s_add_u32 m0, s75, 17408\n" \
    "s_add_u32 s77, s74, 2113536\n" \
    "buffer_load_dwordx4 v29, s[52:55], s77 offen lds\n" \
    "s_add_u32 m0, s75, 18432\n" \
    "s_add_u32 s77, s74, 2129920\n" \
    "buffer_load_dwordx4 v28, s[52:55], s77 offen lds\n" \
    "s_add_u32 m0, s75, 19456\n" \
    "s_add_u32 s77, s74, 2146304\n" \
    "buffer_load_dwordx4 v29, s[52:55], s77 offen lds\n" \
    "s_add_u32 m0, s75, 20480\n" \
    "s_add_u32 s77, s74, 2359296\n" \
    "buffer_load_dwordx4 v28, s[52:55], s77 offen lds\n" \
    "s_add_u32 m0, s75, 21504\n" \
    "s_add_u32 s77, s74, 2375680\n" \
    "buffer_load_dwordx4 v29, s[52:55], s77 offen lds\n" \
    "s_add_u32 m0, s75, 22528\n" \
    "s_add_u32 s77, s74, 2392064\n" \
    "buffer_load_dwordx4 v28, s[52:55], s77 offen lds\n" \
    "s_add_u32 m0, s75, 23552\n" \
    "s_add_u32 s77, s74, 2408448\n" \
    "buffer_load_dwordx4 v29, s[52:55], s77 offen lds\n" \
    "s_add_u32 m0, s75, 24576\n" \
    "s_add_u32 s77, s74, 2162688\n" \
    "buffer_load_dwordx4 v28, s[52:55], s77 offen lds\n" \
    "s_add_u32 m0, s75, 25600\n" \
    "s_add_u32 s77, s74, 2179072\n" \
    "buffer_load_dwordx4 v29, s[52:55], s77 offen lds\n" \
    "s_add_u32 m0, s75, 26624\n" \
    "s_add_u32 s77, s74, 2195456\n" \
    "buffer_load_dwordx4 v28, s[52:55], s77 offen lds\n" \
    "s_add_u32 m0, s75, 27648\n" \
    "s_add_u32 s77, s74, 2211840\n" \
    "buffer_load_dwordx4 v29, s[52:55], s77 offen lds\n" \
    "s_add_u32 m0, s75, 28672\n" \
    "s_add_u32 s77, s74, 2424832\n" \
    "buffer_load_dwordx4 v28, s[52:55], s77 offen lds\n" \
    "s_add_u32 m0, s75, 29696\n" \
    "s_add_u32 s77, s74, 2441216\n" \
    "buffer_load_dwordx4 v29, s[52:55], s77 offen lds\n" \
    "s_add_u32 m0, s75, 30720\n" \
    "s_add_u32 s77, s74, 2457600\n" \
    "buffer_load_dwordx4 v28, s[52:55], s77 offen lds\n" \
    "s_add_u32 m0, s75, 31744\n" \
    "s_add_u32 s77, s74, 2473984\n" \
    "buffer_load_dwordx4 v29, s[52:55], s77 offen lds\n" \
    "s_waitcnt vmcnt(16)\n" \
    "s_barrier\n" \
    "s_xor_b32 s76, s47, 0\n" \
    "s_lshl_b32 s76, s76, 15\n" \
    "v_add_u32 v34, s76, v32\n" \
    "v_xor_b32 v35, 32, v34\n" \
    "ds_read_b64_tr_b16 v[128:129], v34\n" \
    "ds_read_b64_tr_b16 v[130:131], v34 offset:4096\n" \
    "ds_read_b64_tr_b16 v[132:133], v34 offset:8192\n" \
    "ds_read_b64_tr_b16 v[134:135], v34 offset:12288\n" \
    "ds_read_b64_tr_b16 v[192:193], v35\n" \
    "ds_read_b64_tr_b16 v[194:195], v35 offset:4096\n" \
    "ds_read_b64_tr_b16 v[196:197], v35 offset:8192\n" \
    "ds_read_b64_tr_b16 v[198:199], v35 offset:12288\n" \
    "s_xor_b32 s76, s47, 1\n" \
    "s_lshl_b32 s76, s76, 15\n" \
    "v_add_u32 v34, s76, v32\n" \
    "v_xor_b32 v35, 32, v34\n" \
    "ds_read_b64_tr_b16 v[144:145], v34\n" \
    "ds_read_b64_tr_b16 v[146:147], v34 offset:4096\n" \
    "ds_read_b64_tr_b16 v[148:149], v34 offset:8192\n" \
    "ds_read_b64_tr_b16 v[150:151], v34 offset:12288\n" \
    "ds_read_b64_tr_b16 v[208:209], v35\n" \
    "ds_read_b64_tr_b16 v[210:211], v35 offset:4096\n" \
    "ds_read_b64_tr_b16 v[212:213], v35 offset:8192\n" \
    "s_waitcnt lgkmcnt(14)\n" \
    "ds_read_b64_tr_b16 v[214:215], v35 offset:12288\n" \
    "s_xor_b32 s76, s47, 2\n" \
    "s_lshl_b32 s76, s76, 15\n" \
    "v_add_u32 v34, s76, v32\n" \
    "v_xor_b32 v35, 32, v34\n" \
    "s_waitcnt lgkmcnt(14)\n" \
    "ds_read_b64_tr_b16 v[160:161], v34\n" \
    "s_waitcnt lgkmcnt(14)\n" \
    "ds_read_b64_tr_b16 v[162:163], v34 offset:4096\n" \
    "s_waitcnt lgkmcnt(14)\n" \
    "ds_read_b64_tr_b16 v[164:165], v34 offset:8192\n" \
    "s_waitcnt lgkmcnt(14)\n" \
    "ds_read_b64_tr_b16 v[166:167], v34 offset:12288\n" \
    "s_waitcnt lgkmcnt(14)\n" \
    "ds_read_b64_tr_b16 v[224:225], v35\n" \
    "s_waitcnt lgkmcnt(14)\n" \
    "ds_read_b64_tr_b16 v[226:227], v35 offset:4096\n" \
    "s_waitcnt lgkmcnt(14)\n" \
    "ds_read_b64_tr_b16 v[228:229], v35 offset:8192\n" \
    "s_waitcnt lgkmcnt(14)\n" \
    "ds_read_b64_tr_b16 v[230:231], v35 offset:12288\n" \
    "s_xor_b32 s76, s47, 3\n" \
    "s_lshl_b32 s76, s76, 15\n" \
    "v_add_u32 v34, s76, v32\n" \
    "v_xor_b32 v35, 32, v34\n" \
    "s_waitcnt lgkmcnt(14)\n" \
    "ds_read_b64_tr_b16 v[176:177], v34\n" \
    "s_waitcnt lgkmcnt(14)\n" \
    "ds_read_b64_tr_b16 v[178:179], v34 offset:4096\n" \
    "s_waitcnt lgkmcnt(14)\n" \
    "ds_read_b64_tr_b16 v[180:181], v34 offset:8192\n" \
    "s_waitcnt lgkmcnt(14)\n" \
    "ds_read_b64_tr_b16 v[182:183], v34 offset:12288\n" \
    "s_waitcnt lgkmcnt(14)\n" \
    "ds_read_b64_tr_b16 v[240:241], v35\n" \
    "s_waitcnt lgkmcnt(14)\n" \
    "ds_read_b64_tr_b16 v[242:243], v35 offset:4096\n" \
    "s_waitcnt lgkmcnt(14)\n" \
    "ds_read_b64_tr_b16 v[244:245], v35 offset:8192\n" \
    "s_waitcnt lgkmcnt(14)\n" \
    "ds_read_b64_tr_b16 v[246:247], v35 offset:12288\n" \
    "s_waitcnt lgkmcnt(0)\n" \
    "s_barrier\n" \
    "s_mov_b32 s52, s44\n" \
    "s_and_b32 s53, s45, 0xffff\n" \
    "s_mov_b32 s54, 0x20000\n" \
    "s_mov_b32 s55, 0x20000\n" \
    "s_add_u32 m0, s75, 0\n" \
    "s_mov_b32 s77, 0\n" \
    "buffer_load_dwordx4 v30, s[52:55], s77 offen lds\n" \
    "s_add_u32 m0, s75, 1024\n" \
    "s_mov_b32 s77, 16384\n" \
    "buffer_load_dwordx4 v31, s[52:55], s77 offen lds\n" \
    "s_add_u32 m0, s75, 2048\n" \
    "s_mov_b32 s77, 8192\n" \
    "buffer_load_dwordx4 v30, s[52:55], s77 offen lds\n" \
    "s_add_u32 m0, s75, 3072\n" \
    "s_mov_b32 s77, 24576\n" \
    "buffer_load_dwordx4 v31, s[52:55], s77 offen lds\n" \
    "s_add_u32 m0, s75, 4096\n" \
    "s_mov_b32 s77, 2048\n" \
    "buffer_load_dwordx4 v30, s[52:55], s77 offen lds\n" \
    "s_add_u32 m0, s75, 5120\n" \
    "s_mov_b32 s77, 18432\n" \
    "buffer_load_dwordx4 v31, s[52:55], s77 offen lds\n" \
    "s_add_u32 m0, s75, 6144\n" \
    "s_mov_b32 s77, 10240\n" \
    "buffer_load_dwordx4 v30, s[52:55], s77 offen lds\n" \
    "s_add_u32 m0, s75, 7168\n" \
    "s_mov_b32 s77, 26624\n" \
    "buffer_load_dwordx4 v31, s[52:55], s77 offen lds\n" \
    "s_add_u32 m0, s75, 8192\n" \
    "s_mov_b32 s77, 32768\n" \
    "buffer_load_dwordx4 v30, s[52:55], s77 offen lds\n" \
    "s_add_u32 m0, s75, 9216\n" \
    "s_mov_b32 s77, 49152\n" \
    "buffer_load_dwordx4 v31, s[52:55], s77 offen lds\n" \
    "s_add_u32 m0, s75, 10240\n" \
    "s_mov_b32 s77, 40960\n" \
    "buffer_load_dwordx4 v30, s[52:55], s77 offen lds\n" \
    "s_add_u32 m0, s75, 11264\n" \
    "s_mov_b32 s77, 57344\n" \
    "buffer_load_dwordx4 v31, s[52:55], s77 offen lds\n" \
    "s_add_u32 m0, s75, 12288\n" \
    "s_mov_b32 s77, 34816\n" \
    "buffer_load_dwordx4 v30, s[52:55], s77 offen lds\n" \
    "s_add_u32 m0, s75, 13312\n" \
    "s_mov_b32 s77, 51200\n" \
    "buffer_load_dwordx4 v31, s[52:55], s77 offen lds\n" \
    "s_add_u32 m0, s75, 14336\n" \
    "s_mov_b32 s77, 43008\n" \
    "buffer_load_dwordx4 v30, s[52:55], s77 offen lds\n" \
    "s_add_u32 m0, s75, 15360\n" \
    "s_mov_b32 s77, 59392\n" \
    "buffer_load_dwordx4 v31, s[52:55], s77 offen lds\n" \
    "s_waitcnt vmcnt(16)\n" \
    "s_barrier\n" \
    "s_xor_b32 s76, s47, 0\n" \
    "s_lshl_b32 s76, s76, 15\n" \
    "v_add_u32 v34, s76, v32\n" \
    "v_xor_b32 v35, 32, v34\n" \
    "ds_read_b64_tr_b16 v[136:137], v34 offset:16384\n" \
    "ds_read_b64_tr_b16 v[138:139], v34 offset:20480\n" \
    "ds_read_b64_tr_b16 v[140:141], v34 offset:24576\n" \
    "ds_read_b64_tr_b16 v[142:143], v34 offset:28672\n" \
    "ds_read_b64_tr_b16 v[200:201], v35 offset:16384\n" \
    "ds_read_b64_tr_b16 v[202:203], v35 offset:20480\n" \
    "ds_read_b64_tr_b16 v[204:205], v35 offset:24576\n" \
    "ds_read_b64_tr_b16 v[206:207], v35 offset:28672\n" \
    "s_xor_b32 s76, s47, 1\n" \
    "s_lshl_b32 s76, s76, 15\n" \
    "v_add_u32 v34, s76, v32\n" \
    "v_xor_b32 v35, 32, v34\n" \
    "ds_read_b64_tr_b16 v[152:153], v34 offset:16384\n" \
    "ds_read_b64_tr_b16 v[154:155], v34 offset:20480\n" \
    "ds_read_b64_tr_b16 v[156:157], v34 offset:24576\n" \
    "ds_read_b64_tr_b16 v[158:159], v34 offset:28672\n" \
    "ds_read_b64_tr_b16 v[216:217], v35 offset:16384\n" \
    "ds_read_b64_tr_b16 v[218:219], v35 offset:20480\n" \
    "ds_read_b64_tr_b16 v[220:221], v35 offset:24576\n" \
    "s_waitcnt lgkmcnt(14)\n" \
    "ds_read_b64_tr_b16 v[222:223], v35 offset:28672\n" \
    "s_xor_b32 s76, s47, 2\n" \
    "s_lshl_b32 s76, s76, 15\n" \
    "v_add_u32 v34, s76, v32\n" \
    "v_xor_b32 v35, 32, v34\n" \
    "s_waitcnt lgkmcnt(14)\n" \
    "ds_read_b64_tr_b16 v[168:169], v34 offset:16384\n" \
    "s_waitcnt lgkmcnt(14)\n" \
    "ds_read_b64_tr_b16 v[170:171], v34 offset:20480\n" \
    "s_waitcnt lgkmcnt(14)\n" \
    "ds_read_b64_tr_b16 v[172:173], v34 offset:24576\n" \
    "s_waitcnt lgkmcnt(14)\n" \
    "ds_read_b64_tr_b16 v[174:175], v34 offset:28672\n" \
    "s_waitcnt lgkmcnt(14)\n" \
    "ds_read_b64_tr_b16 v[232:233], v35 offset:16384\n" \
    "s_waitcnt lgkmcnt(14)\n" \
    "ds_read_b64_tr_b16 v[234:235], v35 offset:20480\n" \
    "s_waitcnt lgkmcnt(14)\n" \
    "ds_read_b64_tr_b16 v[236:237], v35 offset:24576\n" \
    "s_waitcnt lgkmcnt(14)\n" \
    "ds_read_b64_tr_b16 v[238:239], v35 offset:28672\n" \
    "s_xor_b32 s76, s47, 3\n" \
    "s_lshl_b32 s76, s76, 15\n" \
    "v_add_u32 v34, s76, v32\n" \
    "v_xor_b32 v35, 32, v34\n" \
    "s_waitcnt lgkmcnt(14)\n" \
    "ds_read_b64_tr_b16 v[184:185], v34 offset:16384\n" \
    "s_waitcnt lgkmcnt(14)\n" \
    "ds_read_b64_tr_b16 v[186:187], v34 offset:20480\n" \
    "s_waitcnt lgkmcnt(14)\n" \
    "ds_read_b64_tr_b16 v[188:189], v34 offset:24576\n" \
    "s_waitcnt lgkmcnt(14)\n" \
    "ds_read_b64_tr_b16 v[190:191], v34 offset:28672\n" \
    "s_waitcnt lgkmcnt(14)\n" \
    "ds_read_b64_tr_b16 v[248:249], v35 offset:16384\n" \
    "s_waitcnt lgkmcnt(14)\n" \
    "ds_read_b64_tr_b16 v[250:251], v35 offset:20480\n" \
    "s_waitcnt lgkmcnt(14)\n" \
    "ds_read_b64_tr_b16 v[252:253], v35 offset:24576\n" \
    "s_waitcnt lgkmcnt(14)\n" \
    "ds_read_b64_tr_b16 v[254:255], v35 offset:28672\n" \
    "s_waitcnt lgkmcnt(0)\n" \
    "s_barrier\n" \
    "s_add_u32 m0, s75, 16384\n" \
    "s_mov_b32 s77, 65536\n" \
    "buffer_load_dwordx4 v30, s[52:55], s77 offen lds\n" \
    "s_add_u32 m0, s75, 17408\n" \
    "s_mov_b32 s77, 81920\n" \
    "buffer_load_dwordx4 v31, s[52:55], s77 offen lds\n" \
    "s_add_u32 m0, s75, 18432\n" \
    "s_mov_b32 s77, 73728\n" \
    "buffer_load_dwordx4 v30, s[52:55], s77 offen lds\n" \
    "s_add_u32 m0, s75, 19456\n" \
    "s_mov_b32 s77, 90112\n" \
    "buffer_load_dwordx4 v31, s[52:55], s77 offen lds\n" \
    "s_add_u32 m0, s75, 20480\n" \
    "s_mov_b32 s77, 67584\n" \
    "buffer_load_dwordx4 v30, s[52:55], s77 offen lds\n" \
    "s_add_u32 m0, s75, 21504\n" \
    "s_mov_b32 s77, 83968\n" \
    "buffer_load_dwordx4 v31, s[52:55], s77 offen lds\n" \
    "s_add_u32 m0, s75, 22528\n" \
    "s_mov_b32 s77, 75776\n" \
    "buffer_load_dwordx4 v30, s[52:55], s77 offen lds\n" \
    "s_add_u32 m0, s75, 23552\n" \
    "s_mov_b32 s77, 92160\n" \
    "buffer_load_dwordx4 v31, s[52:55], s77 offen lds\n" \
    "s_add_u32 m0, s75, 24576\n" \
    "s_mov_b32 s77, 98304\n" \
    "buffer_load_dwordx4 v30, s[52:55], s77 offen lds\n" \
    "s_add_u32 m0, s75, 25600\n" \
    "s_mov_b32 s77, 114688\n" \
    "buffer_load_dwordx4 v31, s[52:55], s77 offen lds\n" \
    "s_add_u32 m0, s75, 26624\n" \
    "s_mov_b32 s77, 106496\n" \
    "buffer_load_dwordx4 v30, s[52:55], s77 offen lds\n" \
    "s_add_u32 m0, s75, 27648\n" \
    "s_mov_b32 s77, 122880\n" \
    "buffer_load_dwordx4 v31, s[52:55], s77 offen lds\n" \
    "s_add_u32 m0, s75, 28672\n" \
    "s_mov_b32 s77, 100352\n" \
    "buffer_load_dwordx4 v30, s[52:55], s77 offen lds\n" \
    "s_add_u32 m0, s75, 29696\n" \
    "s_mov_b32 s77, 116736\n" \
    "buffer_load_dwordx4 v31, s[52:55], s77 offen lds\n" \
    "s_add_u32 m0, s75, 30720\n" \
    "s_mov_b32 s77, 108544\n" \
    "buffer_load_dwordx4 v30, s[52:55], s77 offen lds\n" \
    "s_add_u32 m0, s75, 31744\n" \
    "s_mov_b32 s77, 124928\n" \
    "buffer_load_dwordx4 v31, s[52:55], s77 offen lds\n" \
      \
    "v_mov_b32 v19, 0\n" \
    "global_load_dword v17, v19, s[62:63] offset:128 sc1\n" \
    "global_load_dword v18, v19, s[62:63] offset:132 sc1\n" \
    "global_load_dword v32, v19, s[62:63] offset:1152 sc1\n" \
    "global_load_dword v19, v19, s[62:63] offset:1156 sc1\n" \
    "s_waitcnt vmcnt(20)\n" \
    "v_xor_b32 v34, 0, v33\n" \
    "ds_read_b64_tr_b16 v[28:29], v34\n" \
    "ds_read_b64_tr_b16 v[30:31], v34 offset:4096\n" \
    "ds_read_b64_tr_b16 v[100:101], v34 offset:8192\n" \
    "ds_read_b64_tr_b16 v[102:103], v34 offset:12288\n" \
    "v_xor_b32 v35, 32, v33\n" \
    "ds_read_b64_tr_b16 v[104:105], v35\n" \
    "ds_read_b64_tr_b16 v[106:107], v35 offset:4096\n" \
    "ds_read_b64_tr_b16 v[108:109], v35 offset:8192\n" \
    "ds_read_b64_tr_b16 v[110:111], v35 offset:12288\n" \
    "v_xor_b32 v34, 64, v33\n" \
    "ds_read_b64_tr_b16 v[112:113], v34\n" \
    "ds_read_b64_tr_b16 v[114:115], v34 offset:4096\n" \
    "ds_read_b64_tr_b16 v[116:117], v34 offset:8192\n" \
    "ds_read_b64_tr_b16 v[118:119], v34 offset:12288\n" \
    "v_xor_b32 v35, 96, v33\n" \
    "ds_read_b64_tr_b16 v[120:121], v35\n" \
    "ds_read_b64_tr_b16 v[122:123], v35 offset:4096\n" \
    "ds_read_b64_tr_b16 v[124:125], v35 offset:8192\n" \
    "s_waitcnt lgkmcnt(14)\n" \
    "ds_read_b64_tr_b16 v[126:127], v35 offset:12288\n" \
    "v_xor_b32 v34, 128, v33\n" \
    "s_waitcnt lgkmcnt(14)\n" \
    "ds_read_b64_tr_b16 a[0:1], v34\n" \
    "s_waitcnt lgkmcnt(14)\n" \
    "ds_read_b64_tr_b16 a[2:3], v34 offset:4096\n" \
    "s_waitcnt lgkmcnt(14)\n" \
    "ds_read_b64_tr_b16 a[4:5], v34 offset:8192\n" \
    "s_waitcnt lgkmcnt(14)\n" \
    "ds_read_b64_tr_b16 a[6:7], v34 offset:12288\n" \
    "v_xor_b32 v35, 160, v33\n" \
    "s_waitcnt lgkmcnt(14)\n" \
    "ds_read_b64_tr_b16 a[64:65], v35\n" \
    "s_waitcnt lgkmcnt(14)\n" \
    "ds_read_b64_tr_b16 a[66:67], v35 offset:4096\n" \
    "s_waitcnt lgkmcnt(14)\n" \
    "ds_read_b64_tr_b16 a[68:69], v35 offset:8192\n" \
    "s_waitcnt lgkmcnt(14)\n" \
    "ds_read_b64_tr_b16 a[70:71], v35 offset:12288\n" \
    "v_xor_b32 v34, 192, v33\n" \
    "s_waitcnt lgkmcnt(14)\n" \
    "ds_read_b64_tr_b16 a[128:129], v34\n" \
    "s_waitcnt lgkmcnt(14)\n" \
    "ds_read_b64_tr_b16 a[130:131], v34 offset:4096\n" \
    "s_waitcnt lgkmcnt(14)\n" \
    "ds_read_b64_tr_b16 a[132:133], v34 offset:8192\n" \
    "s_waitcnt lgkmcnt(14)\n" \
    "ds_read_b64_tr_b16 a[134:135], v34 offset:12288\n" \
    "v_xor_b32 v35, 224, v33\n" \
    "s_waitcnt lgkmcnt(14)\n" \
    "ds_read_b64_tr_b16 a[192:193], v35\n" \
    "s_waitcnt lgkmcnt(14)\n" \
    "ds_read_b64_tr_b16 a[194:195], v35 offset:4096\n" \
    "s_waitcnt lgkmcnt(14)\n" \
    "ds_read_b64_tr_b16 a[196:197], v35 offset:8192\n" \
    "s_waitcnt lgkmcnt(14)\n" \
    "ds_read_b64_tr_b16 a[198:199], v35 offset:12288\n" \
    "s_waitcnt lgkmcnt(0)\n" \
    "ds_write_b128 v11, v[28:31]\n" \
    "ds_write_b128 v11, v[100:103] offset:1024\n" \
    "ds_write_b128 v11, v[104:107] offset:2048\n" \
    "ds_write_b128 v11, v[108:111] offset:3072\n" \
    "ds_write_b128 v11, v[112:115] offset:4096\n" \
    "ds_write_b128 v11, v[116:119] offset:5120\n" \
    "ds_write_b128 v11, v[120:123] offset:6144\n" \
    "ds_write_b128 v11, v[124:127] offset:7168\n" \
    "ds_write_b128 v11, v[36:39] offset:8192\n" \
    "ds_write_b128 v11, v[40:43] offset:9216\n" \
    "ds_write_b128 v11, v[44:47] offset:10240\n" \
    "ds_write_b128 v11, v[48:51] offset:11264\n" \
    "ds_write_b128 v11, v[52:55] offset:12288\n" \
    "ds_write_b128 v11, v[56:59] offset:13312\n" \
    "ds_write_b128 v11, v[60:63] offset:14336\n" \
    "s_waitcnt lgkmcnt(14)\n" \
    "ds_write_b128 v11, v[64:67] offset:15360\n" \
    "s_waitcnt lgkmcnt(0)\n" \
    "s_waitcnt vmcnt(4)\n" \
    "v_xor_b32 v34, 0, v33\n" \
    "ds_read_b64_tr_b16 v[28:29], v34 offset:16384\n" \
    "ds_read_b64_tr_b16 v[30:31], v34 offset:20480\n" \
    "ds_read_b64_tr_b16 v[100:101], v34 offset:24576\n" \
    "ds_read_b64_tr_b16 v[102:103], v34 offset:28672\n" \
    "v_xor_b32 v35, 32, v33\n" \
    "ds_read_b64_tr_b16 v[104:105], v35 offset:16384\n" \
    "ds_read_b64_tr_b16 v[106:107], v35 offset:20480\n" \
    "ds_read_b64_tr_b16 v[108:109], v35 offset:24576\n" \
    "ds_read_b64_tr_b16 v[110:111], v35 offset:28672\n" \
    "v_xor_b32 v34, 64, v33\n" \
    "ds_read_b64_tr_b16 v[112:113], v34 offset:16384\n" \
    "ds_read_b64_tr_b16 v[114:115], v34 offset:20480\n" \
    "ds_read_b64_tr_b16 v[116:117], v34 offset:24576\n" \
    "ds_read_b64_tr_b16 v[118:119], v34 offset:28672\n" \
    "v_xor_b32 v35, 96, v33\n" \
    "ds_read_b64_tr_b16 v[120:121], v35 offset:16384\n" \
    "ds_read_b64_tr_b16 v[122:123], v35 offset:20480\n" \
    "ds_read_b64_tr_b16 v[124:125], v35 offset:24576\n" \
    "s_waitcnt lgkmcnt(14)\n" \
    "ds_read_b64_tr_b16 v[126:127], v35 offset:28672\n" \
    "v_xor_b32 v34, 128, v33\n" \
    "s_waitcnt lgkmcnt(14)\n" \
    "ds_read_b64_tr_b16 a[8:9], v34 offset:16384\n" \
    "s_waitcnt lgkmcnt(14)\n" \
    "ds_read_b64_tr_b16 a[10:11], v34 offset:20480\n" \
    "s_waitcnt lgkmcnt(14)\n" \
    "ds_read_b64_tr_b16 a[12:13], v34 offset:24576\n" \
    "s_waitcnt lgkmcnt(14)\n" \
    "ds_read_b64_tr_b16 a[14:15], v34 offset:28672\n" \
    "v_xor_b32 v35, 160, v33\n" \
    "s_waitcnt lgkmcnt(14)\n" \
    "ds_read_b64_tr_b16 a[72:73], v35 offset:16384\n" \
    "s_waitcnt lgkmcnt(14)\n" \
    "ds_read_b64_tr_b16 a[74:75], v35 offset:20480\n" \
    "s_waitcnt lgkmcnt(14)\n" \
    "ds_read_b64_tr_b16 a[76:77], v35 offset:24576\n" \
    "s_waitcnt lgkmcnt(14)\n" \
    "ds_read_b64_tr_b16 a[78:79], v35 offset:28672\n" \
    "v_xor_b32 v34, 192, v33\n" \
    "s_waitcnt lgkmcnt(14)\n" \
    "ds_read_b64_tr_b16 a[136:137], v34 offset:16384\n" \
    "s_waitcnt lgkmcnt(14)\n" \
    "ds_read_b64_tr_b16 a[138:139], v34 offset:20480\n" \
    "s_waitcnt lgkmcnt(14)\n" \
    "ds_read_b64_tr_b16 a[140:141], v34 offset:24576\n" \
    "s_waitcnt lgkmcnt(14)\n" \
    "ds_read_b64_tr_b16 a[142:143], v34 offset:28672\n" \
    "v_xor_b32 v35, 224, v33\n" \
    "s_waitcnt lgkmcnt(14)\n" \
    "ds_read_b64_tr_b16 a[200:201], v35 offset:16384\n" \
    "s_waitcnt lgkmcnt(14)\n" \
    "ds_read_b64_tr_b16 a[202:203], v35 offset:20480\n" \
    "s_waitcnt lgkmcnt(14)\n" \
    "ds_read_b64_tr_b16 a[204:205], v35 offset:24576\n" \
    "s_waitcnt lgkmcnt(14)\n" \
    "ds_read_b64_tr_b16 a[206:207], v35 offset:28672\n" \
    "s_waitcnt lgkmcnt(0)\n" \
    "ds_write_b128 v11, v[28:31] offset:16384\n" \
    "ds_write_b128 v11, v[100:103] offset:17408\n" \
    "ds_write_b128 v11, v[104:107] offset:18432\n" \
    "ds_write_b128 v11, v[108:111] offset:19456\n" \
    "ds_write_b128 v11, v[112:115] offset:20480\n" \
    "ds_write_b128 v11, v[116:119] offset:21504\n" \
    "ds_write_b128 v11, v[120:123] offset:22528\n" \
    "ds_write_b128 v11, v[124:127] offset:23552\n" \
    "ds_write_b128 v11, v[68:71] offset:24576\n" \
    "ds_write_b128 v11, v[72:75] offset:25600\n" \
    "ds_write_b128 v11, v[76:79] offset:26624\n" \
    "ds_write_b128 v11, v[80:83] offset:27648\n" \
    "ds_write_b128 v11, v[84:87] offset:28672\n" \
    "ds_write_b128 v11, v[88:91] offset:29696\n" \
    "ds_write_b128 v11, v[92:95] offset:30720\n" \
    "s_waitcnt lgkmcnt(14)\n" \
    "ds_write_b128 v11, v[96:99] offset:31744\n" \
    "s_waitcnt lgkmcnt(0)\n" \
    "ds_write_b128 v2, v[20:23]\n" \
    "s_waitcnt lgkmcnt(0)\n" \
      \
    "s_mov_b32 s71, 131072\n" \
    "s_branch LCSELb%=\n" \
    "LCSEL%=:\n" \
    "v_mov_b32 v19, 0\n" \
    "global_load_dword v17, v19, s[62:63] offset:128 sc1\n" \
    "global_load_dword v18, v19, s[62:63] offset:132 sc1\n" \
    "global_load_dword v32, v19, s[62:63] offset:1152 sc1\n" \
    "global_load_dword v19, v19, s[62:63] offset:1156 sc1\n" \
    "LCSELb%=:\n" \
    "s_waitcnt vmcnt(0)\n" \
    "v_readfirstlane_b32 s64, v17\n" \
    "v_readfirstlane_b32 s65, v18\n" \
    "v_readfirstlane_b32 s72, v32\n" \
    "v_readfirstlane_b32 s76, v19\n" \
    "s_sub_u32 s71, s71, 1\n" \
    "s_cbranch_scc1 LCEND%=\n" \
    "s_cmp_lg_u64 s[64:65], s[96:97]\n" \
    "s_cbranch_scc1 LCSEL%=\n" \
    "s_cmp_lg_u32 s76, s97\n" \
    "s_cbranch_scc1 LCSEL%=\n" \
    "s_and_b32 s76, s72, -8\n" \
    "s_cmp_lg_u32 s76, s96\n" \
    "s_cbranch_scc1 LCSEL%=\n" \
    "s_and_b32 s96, s51, 0xff\n" \
    "s_bfe_u32 s43, s51, 0x80008\n" \
    "s_lshr_b32 s97, s51, 16\n" \
    "v_mov_b32 v35, 1\n" \
    "s_mov_b32 s76, 3145732\n" \
    "s_mov_b64 exec, 1\n" \
    "buffer_atomic_add v35, off, s[56:59], s76\n" \
    "s_mov_b64 exec, -1\n" \
    "s_bfe_u32 s99, s72, 0x10002\n" \
    "s_and_b32 s72, s72, 1\n" \
    "s_sub_u32 s99, s99, s72\n" \
    "s_lshl_b32 s99, s99, 13\n" \
    "s_lshl_b32 s72, s72, 13\n" \
    "s_add_u32 s62, s36, s72\n" \
    "s_addc_u32 s63, s37, 0\n" \
    "s_mov_b32 s52, s62\n" \
    "s_and_b32 s53, s63, 0xffff\n" \
    "s_mov_b32 s54, 0x300000\n" \
    "s_mov_b32 s55, 0x20000\n" \
    "s_mov_b32 s60, 0\n" \
    "s_mov_b32 s61, 0\n" \
    "s_mov_b32 s73, 0x40000\n" \
    "s_add_u32 s78, s99, 2097152\n" \
    "s_lshl_b32 s72, s46, 20\n" \
    "s_lshl_b32 s76, s47, 11\n" \
    "s_add_u32 s72, s72, s76\n" \
    "s_add_u32 s64, s72, 20971520\n" \
    "s_add_u32 s65, s72, 20979712\n" \
    "s_mul_i32 s72, s46, 327680\n" \
    "s_lshl_b32 s77, s47, 9\n" \
    "s_add_u32 s72, s72, s77\n" \
    "s_add_u32 s66, s72, 88080384\n" \
    "s_add_u32 s67, s72, 88082432\n" \
    "s_lshl_b32 s68, s46, 14\n" \
    "s_lshl_b32 s72, s47, 12\n" \
    "s_add_u32 s68, s68, s72\n" \
    "s_lshl_b32 s69, s47, 10\n" \
    "s_add_u32 s63, s69, 154624\n" \
    "s_add_u32 s63, s63, s50\n" \
    "s_add_u32 s69, s69, 2080768\n" \
    "s_add_u32 s69, s69, s99\n" \
    "s_add_u32 s75, s69, 4096\n" \
    "s_add_u32 s70, s76, 131072\n" \
    "s_add_u32 s70, s70, s50\n" \
    "s_mul_i32 s77, s46, 10240\n" \
    "s_add_u32 s77, s77, 3211264\n" \
    "s_lshl_b32 s79, s46, 8\n" \
    "s_add_u32 s79, s79, 3162112\n" \
    "s_mov_b32 s71, 131072\n" \
    "LCKV0%=:\n" \
    "s_sleep 2\n" \
    "buffer_load_dword v17, off, s[56:59], s79 sc1\n" \
    "s_sub_u32 s71, s71, 1\n" \
    "s_cbranch_scc1 LCEND%=\n" \
    "s_waitcnt vmcnt(0)\n" \
    "v_readfirstlane_b32 s72, v17\n" \
    "s_cmp_lt_u32 s72, 4\n" \
    "s_cbranch_scc1 LCKV0%=\n" \
    "s_add_u32 s79, s79, 4\n" \
    "s_mov_b32 s62, 0\n" \
    "s_add_u32 s42, s63, 3072\n" \
    "s_mov_b32 m0, s70\n" \
    "s_nop 0\n" \
    "buffer_load_dwordx4 v0, s[56:59], s64 offen sc1 lds\n" \
    "buffer_load_dwordx4 v0, s[56:59], s64 offen offset:1024 sc1 lds\n" \
    "s_mov_b32 m0, s63\n" \
    "s_nop 0\n" \
    "buffer_load_dwordx4 v0, s[56:59], s65 offen sc1 lds\n" \
    "s_add_u32 m0, s63, 3072\n" \
    "s_nop 0\n" \
    "buffer_load_dwordx4 v0, s[56:59], s65 offen offset:1024 sc1 lds\n" \
    "s_lshl_b32 s74, s47, 10\n" \
    "s_lshl_b32 s51, s46, 14\n" \
    "s_add_u32 s51, s51, s76\n" \
    "s_mov_b32 s41, s51\n" \
    "s_add_u32 s51, s51, 1048576\n" \
    "s_mov_b32 s40, s68\n" \
    "s_cmp_eq_u32 s47, 3\n" \
    "s_cselect_b32 s38, 1, 0\n" \
    "s_mov_b32 s39, 0\n" \
    "s_lshl_b32 s98, s46, 3\n" \
    "s_add_u32 s99, s99, 3129344\n" \
    "s_add_u32 s98, s98, s99\n" \
    "v_and_b32 v124, 16, v0\n" \
    "v_mov_b32 v125, 0x3f80\n" \
    "v_mov_b32 v126, 1.0\n" \
    "v_cmp_eq_u32 vcc, 0, v124\n" \
    "v_and_b32 v124, 0xe0, v0\n" \
    "s_nop 0\n" \
    "v_cndmask_b32 v125, v126, v125, vcc\n" \
    "v_cmp_eq_u32 vcc, 0, v124\n" \
    "s_nop 1\n" \
    "v_cndmask_b32 v108, 0, v125, vcc\n" \
    "v_cmp_eq_u32 vcc, 32, v124\n" \
    "s_nop 1\n" \
    "v_cndmask_b32 v109, 0, v125, vcc\n" \
    "v_cmp_eq_u32 vcc, 64, v124\n" \
    "s_nop 1\n" \
    "v_cndmask_b32 v110, 0, v125, vcc\n" \
    "v_cmp_eq_u32 vcc, 96, v124\n" \
    "s_nop 1\n" \
    "v_cndmask_b32 v111, 0, v125, vcc\n" \
    "ds_write_b128 v8, v[108:111] offset:31744\n" \
    "s_waitcnt lgkmcnt(0)\n" \
    "v_mov_b64 v[52:53], 0\n" \
    "v_mov_b64 v[54:55], 0\n" \
    "v_mov_b64 v[56:57], 0\n" \
    "v_mov_b64 v[58:59], 0\n" \
    "v_mov_b64 v[60:61], 0\n" \
    "v_mov_b64 v[62:63], 0\n" \
    "v_mov_b64 v[64:65], 0\n" \
    "v_mov_b64 v[66:67], 0\n" \
    "v_mov_b64 v[100:101], 0\n" \
    "v_mov_b64 v[102:103], 0\n" \
    "v_mov_b64 v[104:105], 0\n" \
    "v_mov_b64 v[106:107], 0\n" \
    "v_mov_b64 v[108:109], -1\n" \
    "v_mov_b64 v[110:111], -1\n" \
    "ds_read_b128 v[28:31], v11 offset:8192\n" \
    "ds_read_b128 v[32:35], v11 offset:9216\n" \
    "ds_read_b128 v[36:39], v11 offset:24576\n" \
    "ds_read_b128 v[40:43], v11 offset:25600\n" \
    "ds_read_b128 v[44:47], v11 offset:10240\n" \
    "ds_read_b128 v[48:51], v11 offset:11264\n" \
    "ds_read_b128 v[116:119], v11 offset:26624\n" \
    "ds_read_b128 v[120:123], v11 offset:27648\n" \
    "ds_read_b128 v[68:71], v11 offset:12288\n" \
    "ds_read_b128 v[72:75], v11 offset:13312\n" \
    "ds_read_b128 v[76:79], v11 offset:28672\n" \
    "ds_read_b128 v[80:83], v11 offset:29696\n" \
    "ds_read_b128 v[84:87], v11 offset:14336\n" \
    "ds_read_b128 v[88:91], v11 offset:15360\n" \
    "ds_read_b128 v[92:95], v11 offset:30720\n" \
    "s_waitcnt lgkmcnt(14)\n" \
    "ds_read_b128 v[96:99], v11 offset:31744\n" \
    "v_mov_b32 v17, 0\n" \
    "s_waitcnt vmcnt(0)\n" \
    "s_waitcnt lgkmcnt(0)\n" \
    "s_branch LCSTEP0%=\n" \
      \
    "LCP2%=:\n" \
    "s_mov_b32 s72, s43\n" \
    ".p2align 4\n" \
    "LCD0%=:\n" \
    "s_sub_u32 s72, s72, 1\n" \
    "s_cbranch_scc0 LCD0%=\n" \
    "buffer_load_dwordx4 v[24:27], v0, s[52:55], s75 offen sc1\n" \
    "buffer_load_dword v17, off, s[56:59], s79 sc1\n" \
    "s_waitcnt vmcnt(2)\n" \
    "LCP2b%=:\n" \
    "v_smfmac_f32_16x16x64_bf16 v[52:55], v[20:23], v[28:35], v1\n" \
    "ds_write_b128 v2, v[20:23]\n" \
    "v_smfmac_f32_16x16x64_bf16 v[56:59], v[20:23], v[36:43], v1\n" \
    "v_max3_u32 v124, v20, v21, v22\n" \
    "v_smfmac_f32_16x16x64_bf16 v[60:63], v[20:23], v[44:51], v1\n" \
    "v_max_u32 v124, v124, v23\n" \
    "v_smfmac_f32_16x16x64_bf16 v[64:67], v[20:23], v[116:123], v1\n" \
    "v_cmp_eq_u32 vcc, -1, v124\n" \
    "s_cbranch_vccnz LCP2R%=\n" \
    "LCSTEP%=:\n" \
    "s_waitcnt lgkmcnt(0)\n" \
    "s_barrier\n" \
      \
    "ds_read_b128 v[28:31], v3\n" \
    "ds_read_b128 v[36:39], v4\n" \
    "v_smfmac_f32_16x16x64_bf16 v[100:103], v[20:23], v[128:135], v1\n" \
    "ds_read_b128 v[44:47], v5\n" \
    "s_add_u32 s69, s69, 16384\n" \
    "v_smfmac_f32_16x16x64_bf16 v[104:107], v[20:23], v[192:199], v1\n" \
    "v_add_u32 v126, s74, v7\n" \
    "ds_read_b64_tr_b16 v[124:125], v126\n" \
    "ds_read_b64_tr_b16 v[126:127], v126 offset:512\n" \
    "s_waitcnt lgkmcnt(4)\n" \
    "v_smfmac_f32_16x16x64_bf16 v[52:55], v[28:31], a[16:23], v1\n" \
    "v_smfmac_f32_16x16x64_bf16 v[56:59], v[28:31], a[144:151], v1\n" \
    "v_smfmac_f32_16x16x64_bf16 v[60:63], v[28:31], a[80:87], v1\n" \
    "s_waitcnt vmcnt(0)\n" \
    "LCP3b%=:\n" \
    "v_smfmac_f32_16x16x64_bf16 v[64:67], v[28:31], a[208:215], v1\n" \
    "ds_write_b128 v2, v[24:27] offset:4096\n" \
    "v_max3_u32 v116, v24, v25, v26\n" \
    "s_waitcnt lgkmcnt(4)\n" \
    "v_smfmac_f32_16x16x64_bf16 v[52:55], v[36:39], a[32:39], v1\n" \
    "v_max_u32 v116, v116, v27\n" \
    "v_smfmac_f32_16x16x64_bf16 v[56:59], v[36:39], a[160:167], v1\n" \
    "v_cmp_eq_u32 vcc, -1, v116\n" \
    "s_cbranch_vccnz LCP3R%=\n" \
    "LCP3c%=:\n" \
    "v_smfmac_f32_16x16x64_bf16 v[60:63], v[36:39], a[96:103], v1\n" \
    "v_readfirstlane_b32 s62, v17\n" \
    "v_smfmac_f32_16x16x64_bf16 v[64:67], v[36:39], a[224:231], v1\n" \
    "s_add_u32 s75, s75, 16384\n" \
    "s_waitcnt lgkmcnt(0)\n" \
    "s_barrier\n" \
    "ds_read_b128 v[32:35], v3 offset:4096\n" \
    "ds_read_b128 v[40:43], v4 offset:4096\n" \
    "v_smfmac_f32_16x16x64_bf16 v[52:55], v[44:47], a[48:55], v1\n" \
    "ds_read_b128 v[48:51], v5 offset:4096\n" \
    "v_smfmac_f32_16x16x64_bf16 v[56:59], v[44:47], a[176:183], v1\n" \
    "buffer_store_dwordx4 v[108:111], v0, s[52:55], s51 offen sc1\n" \
    "v_smfmac_f32_16x16x64_bf16 v[60:63], v[44:47], a[112:119], v1\n" \
    "s_mov_b64 exec, s[38:39]\n" \
    "s_cbranch_execz LCNF%=\n" \
    "buffer_store_dword v1, off, s[56:59], s77 sc1\n" \
    "LCNF%=:\n" \
    "s_mov_b64 exec, -1\n" \
    "s_add_u32 s77, s77, 128\n" \
    "v_smfmac_f32_16x16x64_bf16 v[64:67], v[44:47], a[240:247], v1\n" \
    "v_smfmac_f32_16x16x64_bf16 v[52:55], v[24:27], v[68:75], v1\n" \
    "v_smfmac_f32_16x16x64_bf16 v[56:59], v[24:27], v[76:83], v1\n" \
    "buffer_store_dwordx2 v[18:19], v15, s[56:59], s67 offen sc1\n" \
    "v_smfmac_f32_16x16x64_bf16 v[60:63], v[24:27], v[84:91], v1\n" \
    "ds_read_b128 v[68:71], v8\n" \
    "v_smfmac_f32_16x16x64_bf16 v[64:67], v[24:27], v[92:99], v1\n" \
    "ds_read_b128 v[72:75], v8 offset:4096\n" \
    "s_waitcnt lgkmcnt(4)\n" \
    "v_smfmac_f32_16x16x64_bf16 v[52:55], v[32:35], a[24:31], v1\n" \
    "ds_read_b128 v[76:79], v8 offset:1024\n" \
    "v_smfmac_f32_16x16x64_bf16 v[56:59], v[32:35], a[152:159], v1\n" \
    "ds_read_b128 v[80:83], v8 offset:5120\n" \
    "buffer_store_dwordx4 v[124:127], v14, s[56:59], s66 offen sc1\n" \
    "s_waitcnt lgkmcnt(4)\n" \
    "v_smfmac_f32_16x16x64_bf16 v[52:55], v[40:43], a[40:47], v1\n" \
    "ds_read_b128 v[84:87], v8 offset:2048\n" \
    "v_smfmac_f32_16x16x64_bf16 v[56:59], v[40:43], a[168:175], v1\n" \
    "ds_read_b128 v[88:91], v8 offset:6144\n" \
    "buffer_store_dwordx4 v[108:111], v0, s[52:55], s51 offen offset:1024 sc1\n" \
    "v_smfmac_f32_16x16x64_bf16 v[52:55], v[48:51], a[56:63], v1\n" \
    "ds_read_b128 v[92:95], v8 offset:3072\n" \
    "s_add_u32 s66, s66, 4096\n" \
    "v_smfmac_f32_16x16x64_bf16 v[56:59], v[48:51], a[184:191], v1\n" \
    "ds_read_b128 v[96:99], v8 offset:7168\n" \
    "s_add_u32 s67, s67, 4096\n" \
    "v_smfmac_f32_16x16x64_bf16 v[60:63], v[32:35], a[88:95], v1\n" \
    "ds_read_b128 v[108:111], v2 offset:15360\n" \
    "s_cmp_eq_u32 s60, 63\n" \
    "s_cselect_b32 s72, 0, 0x4000\n" \
    "v_smfmac_f32_16x16x64_bf16 v[64:67], v[32:35], a[216:223], v1\n" \
    "ds_read_b128 v[112:115], v2 offset:19456\n" \
    "s_cmov_b32 s62, 4\n" \
    "s_add_u32 s64, s64, s72\n" \
    "v_pk_add_f32 v[52:53], v[52:53], v[54:55]\n" \
    "v_smfmac_f32_16x16x64_bf16 v[60:63], v[40:43], a[104:111], v1\n" \
    "s_add_u32 s65, s65, s72\n" \
    "v_pk_add_f32 v[56:57], v[56:57], v[58:59]\n" \
    "s_cmp_lt_u32 s62, 4\n" \
    "v_smfmac_f32_16x16x64_bf16 v[64:67], v[40:43], a[232:239], v1\n" \
    "v_cvt_pk_bf16_f32 v55, v52, v53\n" \
    "s_cbranch_scc1 LCKVSLOW%=\n" \
    "LCKVOK%=:\n" \
    "v_smfmac_f32_16x16x64_bf16 v[60:63], v[48:51], a[120:127], v1\n" \
    "v_cvt_pk_bf16_f32 v54, v56, v57\n" \
    "s_waitcnt lgkmcnt(0)\n" \
    "s_mov_b32 m0, s63\n" \
    "s_nop 0\n" \
    "buffer_load_dwordx4 v0, s[56:59], s65 offen sc1 lds\n" \
    "v_smfmac_f32_16x16x64_bf16 v[64:67], v[48:51], a[248:255], v1\n" \
      \
    "ds_write_b64 v6, v[54:55]\n" \
    "v_smfmac_f32_16x16x64_bf16 v[100:103], v[28:31], v[144:151], v1\n" \
    "s_mov_b32 m0, s70\n" \
    "s_memrealtime s[48:49]\n" \
    "v_smfmac_f32_16x16x64_bf16 v[104:107], v[28:31], v[208:215], v1\n" \
    "s_nop 0\n" \
    "v_pk_add_f32 v[60:61], v[60:61], v[62:63]\n" \
    "v_smfmac_f32_16x16x64_bf16 v[100:103], v[36:39], v[160:167], v1\n" \
    "v_pk_add_f32 v[64:65], v[64:65], v[66:67]\n" \
    "v_cvt_pk_bf16_f32 v63, v60, v61\n" \
    "v_cvt_pk_bf16_f32 v62, v64, v65\n" \
    "ds_write_b64 v6, v[62:63] offset:512\n" \
    "v_smfmac_f32_16x16x64_bf16 v[104:107], v[36:39], v[224:231], v1\n" \
    "s_mov_b32 m0, s42\n" \
    "s_nop 0\n" \
    "buffer_load_dwordx4 v0, s[56:59], s65 offen offset:1024 sc1 lds\n" \
    "v_smfmac_f32_16x16x64_bf16 v[100:103], v[44:47], v[176:183], v1\n" \
    "v_smfmac_f32_16x16x64_bf16 v[104:107], v[44:47], v[240:247], v1\n" \
    "s_waitcnt lgkmcnt(0)\n" \
    "s_barrier\n" \
    "ds_read_b64_tr_b16 v[52:53], v7\n" \
    "ds_read_b64_tr_b16 v[54:55], v7 offset:512\n" \
    "v_smfmac_f32_16x16x64_bf16 v[100:103], v[24:27], v[136:143], v1\n" \
    "ds_read_b64_tr_b16 v[56:57], v7 offset:1024\n" \
    "ds_read_b64_tr_b16 v[58:59], v7 offset:1536\n" \
    "v_smfmac_f32_16x16x64_bf16 v[104:107], v[24:27], v[200:207], v1\n" \
    "ds_read_b64_tr_b16 v[60:61], v7 offset:2048\n" \
    "ds_read_b64_tr_b16 v[62:63], v7 offset:2560\n" \
    "v_smfmac_f32_16x16x64_bf16 v[100:103], v[32:35], v[152:159], v1\n" \
    "ds_read_b64_tr_b16 v[64:65], v7 offset:3072\n" \
    "ds_read_b64_tr_b16 v[66:67], v7 offset:3584\n" \
    "v_smfmac_f32_16x16x64_bf16 v[104:107], v[32:35], v[216:223], v1\n" \
    "s_mov_b32 m0, s70\n" \
    "s_nop 0\n" \
    "buffer_load_dwordx4 v0, s[56:59], s64 offen sc1 lds\n" \
    "v_mov_b32 v17, s48\n" \
    "v_smfmac_f32_16x16x64_bf16 v[100:103], v[40:43], v[168:175], v1\n" \
    "v_smfmac_f32_16x16x64_bf16 v[104:107], v[40:43], v[232:239], v1\n" \
      \
    "s_waitcnt lgkmcnt(6)\n" \
    "v_mfma_f32_16x16x32_bf16 v[116:119], v[68:71], v[52:55], 0\n" \
    "s_mov_b64 exec, s[38:39]\n" \
    "s_cbranch_execz LCND%=\n" \
    "buffer_store_dword v17, off, s[52:55], s98 sc1\n" \
    "LCND%=:\n" \
    "s_mov_b64 exec, -1\n" \
    "v_mfma_f32_16x16x32_bf16 v[120:123], v[72:75], v[52:55], 0\n" \
    "s_mov_b32 m0, s70\n" \
    "s_nop 0\n" \
    "buffer_load_dwordx4 v0, s[56:59], s64 offen offset:1024 sc1 lds\n" \
    "s_xor_b32 s98, s98, 256\n" \
    "v_mfma_f32_16x16x32_bf16 v[124:127], v[52:55], v[52:55], 0\n" \
    "ds_read_b128 v[20:23], v11\n" \
    "ds_read_b128 v[24:27], v11 offset:1024\n" \
    "s_waitcnt lgkmcnt(6)\n" \
    "v_mfma_f32_16x16x32_bf16 v[116:119], v[76:79], v[56:59], v[116:119]\n" \
    "ds_read_b128 v[28:31], v11 offset:2048\n" \
    "ds_read_b128 v[32:35], v11 offset:3072\n" \
    "v_mfma_f32_16x16x32_bf16 v[120:123], v[80:83], v[56:59], v[120:123]\n" \
    "v_mfma_f32_16x16x32_bf16 v[124:127], v[56:59], v[56:59], v[124:127]\n" \
    "ds_read_b128 v[68:71], v11 offset:20480\n" \
    "ds_read_b128 v[72:75], v11 offset:21504\n" \
    "s_waitcnt lgkmcnt(8)\n" \
    "v_mfma_f32_16x16x32_bf16 v[116:119], v[84:87], v[60:63], v[116:119]\n" \
    "ds_read_b128 v[76:79], v11 offset:22528\n" \
    "ds_read_b128 v[80:83], v11 offset:23552\n" \
    "v_mfma_f32_16x16x32_bf16 v[120:123], v[88:91], v[60:63], v[120:123]\n" \
    "v_mfma_f32_16x16x32_bf16 v[124:127], v[60:63], v[60:63], v[124:127]\n" \
    "ds_read_b128 v[52:55], v11 offset:16384\n" \
    "ds_read_b128 v[56:59], v11 offset:17408\n" \
    "s_waitcnt lgkmcnt(10)\n" \
    "v_mfma_f32_16x16x32_bf16 v[124:127], v[64:67], v[64:67], v[124:127]\n" \
    "v_mov_b32 v89, 0\n" \
    "v_mov_b64 v[90:91], 0\n" \
    "s_cmp_eq_u32 s60, 63\n" \
    "v_mfma_f32_16x16x32_bf16 v[116:119], v[92:95], v[64:67], v[116:119]\n" \
    "v_mov_b32 v92, 0x3f803f80\n" \
    "v_mov_b32 v93, 0x3f803f80\n" \
    "v_mfma_f32_16x16x32_bf16 v[120:123], v[96:99], v[64:67], v[120:123]\n" \
    "v_smfmac_f32_16x16x64_bf16 v[100:103], v[48:51], v[184:191], v1\n" \
    "ds_read_b128 v[36:39], v11 offset:4096\n" \
    "ds_read_b128 v[40:43], v11 offset:5120\n" \
    "v_smfmac_f32_16x16x64_bf16 v[104:107], v[48:51], v[248:255], v1\n" \
    "ds_read_b128 v[60:63], v11 offset:18432\n" \
    "ds_read_b128 v[64:67], v11 offset:19456\n" \
    "s_cbranch_scc1 LCLAST%=\n" \
    "v_mov_b32 v94, 0x3f803f80\n" \
    "v_mov_b32 v95, 0x3f803f80\n" \
      \
    "v_maximum3_f32 v17, v124, v125, v116\n" \
    "v_maximum3_f32 v18, v117, v118, v119\n" \
    "ds_read_b128 v[44:47], v11 offset:6144\n" \
    "s_waitcnt lgkmcnt(14)\n" \
    "ds_read_b128 v[48:51], v11 offset:7168\n" \
    "v_maximum3_f32 v19, v120, v121, v122\n" \
    "v_maximum3_f32 v17, v17, v18, v123\n" \
    "v_maximum3_f32 v18, v17, v19, v19\n" \
    "v_maximum3_f32 v17, v17, v19, v19\n" \
    "v_pk_add_f32 v[100:101], v[100:101], v[102:103]\n" \
    "v_pk_add_f32 v[104:105], v[104:105], v[106:107]\n" \
    "v_permlane16_swap_b32 v17, v18\n" \
    "v_maximum3_f32 v19, v17, v18, v18\n" \
    "v_maximum3_f32 v18, v17, v18, v18\n" \
    "v_cvt_pk_bf16_f32 v100, v100, v101\n" \
    "v_cvt_pk_bf16_f32 v101, v104, v105\n" \
    "v_permlane32_swap_b32 v19, v18\n" \
    "v_maximum3_f32 v17, v19, v18, v18\n" \
    "v_mul_f32 v17, 0xbe0293ee, v17\n" \
    "v_fmamk_f32 v124, v124, 0x3e0293ee, v17\n" \
    "v_fmamk_f32 v125, v125, 0x3e0293ee, v17\n" \
    "v_exp_f32 v124, v124\n" \
    "v_exp_f32 v125, v125\n" \
    "v_fmamk_f32 v116, v116, 0x3e0293ee, v17\n" \
    "v_fmamk_f32 v117, v117, 0x3e0293ee, v17\n" \
    "v_cvt_pk_bf16_f32 v88, v124, v125\n" \
    "v_exp_f32 v116, v116\n" \
    "v_exp_f32 v117, v117\n" \
    "v_mfma_f32_16x16x32_bf16 v[96:99], v[92:95], v[88:91], 0\n" \
    "v_mov_b64 v[102:103], 0\n" \
    "v_mov_b32 v104, 0\n" \
    "v_mov_b64 v[106:107], 0\n" \
    "v_mov_b32 v105, v88\n" \
    "v_mfma_f32_16x16x32_bf16 v[124:127], v[100:103], v[88:91], 0\n" \
    "v_fmamk_f32 v118, v118, 0x3e0293ee, v17\n" \
    "v_fmamk_f32 v119, v119, 0x3e0293ee, v17\n" \
    "v_exp_f32 v118, v118\n" \
    "v_exp_f32 v119, v119\n" \
    "v_mfma_f32_16x16x32_bf16 v[84:87], v[100:103], v[104:107], 0\n" \
    "v_cvt_pk_bf16_f32 v116, v116, v117\n" \
    "v_fmamk_f32 v120, v120, 0x3e0293ee, v17\n" \
    "v_fmamk_f32 v121, v121, 0x3e0293ee, v17\n" \
    "v_cvt_pk_bf16_f32 v117, v118, v119\n" \
    "v_exp_f32 v120, v120\n" \
    "v_exp_f32 v121, v121\n" \
    "v_fmamk_f32 v122, v122, 0x3e0293ee, v17\n" \
    "v_fmamk_f32 v123, v123, 0x3e0293ee, v17\n" \
    "v_exp_f32 v122, v122\n" \
    "v_exp_f32 v123, v123\n" \
    "v_cvt_pk_bf16_f32 v118, v120, v121\n" \
    "v_cvt_pk_bf16_f32 v119, v122, v123\n" \
    "s_nop 1\n" \
    "v_mfma_f32_16x16x32_bf16 v[96:99], v[92:95], v[116:119], v[96:99]\n" \
    "v_mfma_f32_16x16x32_bf16 v[124:127], v[108:111], v[116:119], v[124:127]\n" \
    "v_mfma_f32_16x16x32_bf16 v[84:87], v[112:115], v[116:119], v[84:87]\n" \
    "v_mov_b64 v[102:103], 0\n" \
    "v_mov_b64 v[104:105], 0\n" \
    "v_mov_b64 v[88:89], 0\n" \
    "v_mov_b64 v[90:91], 0\n" \
    "v_mov_b64 v[92:93], 0\n" \
    "s_nop 0\n" \
    "v_rcp_f32 v96, v96\n" \
    "v_mov_b64 v[18:19], v[100:101]\n" \
    "v_mul_f32 v124, v96, v124\n" \
    "v_mul_f32 v125, v96, v125\n" \
    "v_mul_f32 v126, v96, v126\n" \
    "v_mul_f32 v127, v96, v127\n" \
    "v_cvt_pk_bf16_f32 v124, v124, v125\n" \
    "v_cvt_pk_bf16_f32 v125, v126, v127\n" \
    "v_mul_f32 v84, v96, v84\n" \
    "v_mul_f32 v85, v96, v85\n" \
    "v_mul_f32 v86, v96, v86\n" \
    "v_mul_f32 v87, v96, v87\n" \
    "v_cvt_pk_bf16_f32 v126, v84, v85\n" \
    "v_cvt_pk_bf16_f32 v127, v86, v87\n" \
      \
    "s_waitcnt lgkmcnt(14)\n" \
    "ds_write_b128 v9, v[124:127]\n" \
    "v_mov_b64 v[94:95], 0\n" \
    "v_mov_b64 v[100:101], 0\n" \
    "v_mov_b64 v[106:107], 0\n" \
    "v_mov_b64 v[108:109], 0\n" \
    "v_mov_b64 v[110:111], 0\n" \
    "v_mov_b64 v[112:113], 0\n" \
    "v_mov_b64 v[114:115], 0\n" \
    "s_waitcnt lgkmcnt(0)\n" \
    "s_barrier\n" \
    "ds_read_b128 v[124:127], v10\n" \
    "ds_read_b128 v[84:87], v10 offset:1024\n" \
    "buffer_load_dword v17, v15, s[52:55], s99 offen sc1\n" \
    "v_mov_b64 v[116:117], 0\n" \
    "v_mov_b64 v[118:119], 0\n" \
    "v_mov_b64 v[120:121], 0\n" \
    "v_mov_b64 v[122:123], 0\n" \
    "s_waitcnt lgkmcnt(1)\n" \
    "v_smfmac_f32_16x16x64_bf16 v[88:91], v[124:127], v[20:27], v1\n" \
    "v_smfmac_f32_16x16x64_bf16 v[92:95], v[124:127], v[28:35], v1\n" \
    "s_waitcnt lgkmcnt(0)\n" \
    "v_smfmac_f32_16x16x64_bf16 v[88:91], v[84:87], v[52:59], v1\n" \
    "v_smfmac_f32_16x16x64_bf16 v[92:95], v[84:87], v[60:67], v1\n" \
    "v_smfmac_f32_16x16x64_bf16 v[100:103], v[124:127], v[36:43], v1\n" \
    "v_smfmac_f32_16x16x64_bf16 v[104:107], v[124:127], v[44:51], v1\n" \
    "s_nop 1\n" \
    "v_smfmac_f32_16x16x64_bf16 v[100:103], v[84:87], v[68:75], v1\n" \
    "s_nop 0\n" \
    "v_smfmac_f32_16x16x64_bf16 v[104:107], v[84:87], v[76:83], v1\n" \
    "v_pk_add_f32 v[88:89], v[88:89], v[90:91]\n" \
    "v_pk_add_f32 v[92:93], v[92:93], v[94:95]\n" \
    "v_cvt_pk_bf16_f32 v20, v88, v92\n" \
    "v_cvt_pk_bf16_f32 v22, v89, v93\n" \
    "v_smfmac_f32_16x16x64_bf16 v[108:111], v[124:127], a[0:7], v1\n" \
    "s_nop 0\n" \
    "v_pk_add_f32 v[100:101], v[100:101], v[102:103]\n" \
    "s_nop 0\n" \
    "v_pk_add_f32 v[104:105], v[104:105], v[106:107]\n" \
    "v_cvt_pk_bf16_f32 v21, v100, v104\n" \
    "v_cvt_pk_bf16_f32 v23, v101, v105\n" \
    "buffer_store_dwordx4 v[20:23], v12, s[52:55], s61 offen sc1\n" \
    "v_smfmac_f32_16x16x64_bf16 v[112:115], v[124:127], a[64:71], v1\n" \
    "v_smfmac_f32_16x16x64_bf16 v[108:111], v[84:87], a[8:15], v1\n" \
    "v_smfmac_f32_16x16x64_bf16 v[112:115], v[84:87], a[72:79], v1\n" \
    "v_smfmac_f32_16x16x64_bf16 v[116:119], v[124:127], a[128:135], v1\n" \
    "s_nop 1\n" \
    "v_smfmac_f32_16x16x64_bf16 v[120:123], v[124:127], a[192:199], v1\n" \
    "s_nop 0\n" \
    "s_nop 1\n" \
    "v_pk_add_f32 v[108:109], v[108:109], v[110:111]\n" \
    "v_smfmac_f32_16x16x64_bf16 v[116:119], v[84:87], a[136:143], v1\n" \
    "v_pk_add_f32 v[112:113], v[112:113], v[114:115]\n" \
    "v_smfmac_f32_16x16x64_bf16 v[120:123], v[84:87], a[200:207], v1\n" \
    "v_cvt_pk_bf16_f32 v24, v108, v112\n" \
    "v_cvt_pk_bf16_f32 v26, v109, v113\n" \
    "s_nop 3\n" \
    "v_pk_add_f32 v[116:117], v[116:117], v[118:119]\n" \
    "s_nop 0\n" \
    "v_pk_add_f32 v[120:121], v[120:121], v[122:123]\n" \
    "v_cvt_pk_bf16_f32 v25, v116, v120\n" \
    "v_cvt_pk_bf16_f32 v27, v117, v121\n" \
    "buffer_store_dwordx4 v[24:27], v12, s[52:55], s73 offen sc1\n" \
    "s_cmp_lt_u32 s47, 2\n" \
    "s_cbranch_scc1 LCRED%=\n" \
    "ds_read_b128 v[28:31], v11 offset:8192\n" \
    "ds_read_b128 v[32:35], v11 offset:9216\n" \
    "ds_read_b128 v[36:39], v11 offset:24576\n" \
    "ds_read_b128 v[40:43], v11 offset:25600\n" \
    "ds_read_b128 v[44:47], v11 offset:10240\n" \
    "ds_read_b128 v[48:51], v11 offset:11264\n" \
    "ds_read_b128 v[116:119], v11 offset:26624\n" \
    "ds_read_b128 v[120:123], v11 offset:27648\n" \
    "ds_read_b128 v[68:71], v11 offset:12288\n" \
    "ds_read_b128 v[72:75], v11 offset:13312\n" \
    "ds_read_b128 v[76:79], v11 offset:28672\n" \
    "ds_read_b128 v[80:83], v11 offset:29696\n" \
    "ds_read_b128 v[84:87], v11 offset:14336\n" \
    "ds_read_b128 v[88:91], v11 offset:15360\n" \
    "ds_read_b128 v[92:95], v11 offset:30720\n" \
    "s_waitcnt lgkmcnt(14)\n" \
    "ds_read_b128 v[96:99], v11 offset:31744\n" \
    "s_waitcnt vmcnt(0)\n" \
    "s_branch LCB5%=\n" \
      \
    "LCRED%=:\n" \
    "ds_read_b128 v[108:111], v8 offset:31744\n" \
    "s_waitcnt vmcnt(2)\n" \
    "v_subrev_u32 v17, s48, v17\n" \
    "v_med3_i32 v17, v17, 0, 40\n" \
    "s_nop 1\n" \
    "v_max_i32_dpp v17, v17, v17 row_ror:1 row_mask:0xf bank_mask:0xf bound_ctrl:1\n" \
    "s_nop 1\n" \
    "v_max_i32_dpp v17, v17, v17 row_ror:2 row_mask:0xf bank_mask:0xf bound_ctrl:1\n" \
    "s_nop 1\n" \
    "v_max_i32_dpp v17, v17, v17 row_ror:4 row_mask:0xf bank_mask:0xf bound_ctrl:1\n" \
    "s_nop 1\n" \
    "v_max_i32_dpp v17, v17, v17 row_ror:8 row_mask:0xf bank_mask:0xf bound_ctrl:1\n" \
    "s_nop 1\n" \
    "v_readlane_b32 s72, v17, 0\n" \
    "v_readlane_b32 s76, v17, 16\n" \
    "v_readlane_b32 s48, v17, 32\n" \
    "v_readlane_b32 s49, v17, 48\n" \
    "s_max_u32 s72, s72, s76\n" \
    "s_max_u32 s48, s48, s49\n" \
    "s_max_u32 s72, s72, s48\n" \
    "s_mul_i32 s72, s72, 10\n" \
    "s_lshr_b32 s72, s72, 4\n" \
    "s_add_u32 s48, s72, s96\n" \
    "s_mov_b32 s72, s48\n" \
    ".p2align 4\n" \
    "LCD1%=:\n" \
    "s_sub_u32 s72, s72, 1\n" \
    "s_cbranch_scc0 LCD1%=\n" \
    "s_mov_b32 s71, 131072\n" \
    "buffer_load_dwordx4 v[52:55], v0, s[52:55], s68 offen sc1\n" \
    "buffer_load_dwordx4 v[56:59], v0, s[52:55], s68 offen offset:1024 sc1\n" \
    "buffer_load_dwordx4 v[60:63], v0, s[52:55], s68 offen offset:2048 sc1\n" \
    "buffer_load_dwordx4 v[64:67], v0, s[52:55], s68 offen offset:3072 sc1\n" \
    "ds_read_b128 v[28:31], v11 offset:8192\n" \
    "ds_read_b128 v[32:35], v11 offset:9216\n" \
    "ds_read_b128 v[36:39], v11 offset:24576\n" \
    "ds_read_b128 v[40:43], v11 offset:25600\n" \
    "ds_read_b128 v[44:47], v11 offset:10240\n" \
    "ds_read_b128 v[48:51], v11 offset:11264\n" \
    "ds_read_b128 v[116:119], v11 offset:26624\n" \
    "ds_read_b128 v[120:123], v11 offset:27648\n" \
    "ds_read_b128 v[68:71], v11 offset:12288\n" \
    "ds_read_b128 v[72:75], v11 offset:13312\n" \
    "ds_read_b128 v[76:79], v11 offset:28672\n" \
    "ds_read_b128 v[80:83], v11 offset:29696\n" \
    "ds_read_b128 v[84:87], v11 offset:14336\n" \
    "ds_read_b128 v[88:91], v11 offset:15360\n" \
    "s_waitcnt lgkmcnt(14)\n" \
    "ds_read_b128 v[92:95], v11 offset:30720\n" \
    "s_waitcnt lgkmcnt(14)\n" \
    "ds_read_b128 v[96:99], v11 offset:31744\n" \
    "s_branch LCP1b%=\n" \
    "LCP1%=:\n" \
    "buffer_load_dwordx4 v[52:55], v0, s[52:55], s68 offen sc1\n" \
    "buffer_load_dwordx4 v[56:59], v0, s[52:55], s68 offen offset:1024 sc1\n" \
    "buffer_load_dwordx4 v[60:63], v0, s[52:55], s68 offen offset:2048 sc1\n" \
    "buffer_load_dwordx4 v[64:67], v0, s[52:55], s68 offen offset:3072 sc1\n" \
    "s_sub_u32 s71, s71, 1\n" \
    "s_cbranch_scc1 LCEND%=\n" \
    "LCP1b%=:\n" \
    "s_waitcnt vmcnt(3)\n" \
    "v_max_u32 v124, v52, v53\n" \
    "v_max3_u32 v124, v54, v55, v124\n" \
    "v_mfma_f32_16x16x32_bf16 v[112:115], v[108:111], v[52:55], 0\n" \
    "s_waitcnt vmcnt(2)\n" \
    "v_max3_u32 v124, v56, v57, v124\n" \
    "v_max3_u32 v124, v58, v59, v124\n" \
    "v_mfma_f32_16x16x32_bf16 v[112:115], v[108:111], v[56:59], v[112:115]\n" \
    "s_waitcnt vmcnt(1)\n" \
    "v_max3_u32 v124, v60, v61, v124\n" \
    "v_max3_u32 v124, v62, v63, v124\n" \
    "v_mfma_f32_16x16x32_bf16 v[112:115], v[108:111], v[60:63], v[112:115]\n" \
    "s_waitcnt vmcnt(0)\n" \
    "v_max3_u32 v124, v64, v65, v124\n" \
    "v_max3_u32 v124, v66, v67, v124\n" \
    "v_mfma_f32_16x16x32_bf16 v[112:115], v[108:111], v[64:67], v[112:115]\n" \
    "v_cmp_eq_u32 vcc, -1, v124\n" \
    "s_cbranch_vccnz LCP1%=\n" \
    "s_nop 5\n" \
    "v_add_f32_dpp v112, v112, v112 row_ror:8 row_mask:0xf bank_mask:0xf bound_ctrl:1\n" \
    "v_add_f32_dpp v113, v113, v113 row_ror:8 row_mask:0xf bank_mask:0xf bound_ctrl:1\n" \
    "v_add_f32_dpp v114, v114, v114 row_ror:8 row_mask:0xf bank_mask:0xf bound_ctrl:1\n" \
    "v_add_f32_dpp v115, v115, v115 row_ror:8 row_mask:0xf bank_mask:0xf bound_ctrl:1\n" \
    "v_cvt_pk_bf16_f32 v126, v112, v113\n" \
    "v_cvt_pk_bf16_f32 v127, v114, v115\n" \
    "buffer_store_dwordx2 v[126:127], v13, s[52:55], s78 offen sc1\n" \
    "s_nop 0\n" \
    "LCB5%=:\n" \
    "s_barrier\n" \
    "s_mov_b32 s72, s97\n" \
    ".p2align 4\n" \
    "LCD2%=:\n" \
    "s_sub_u32 s72, s72, 1\n" \
    "s_cbranch_scc0 LCD2%=\n" \
    "buffer_load_dwordx4 v[20:23], v0, s[52:55], s69 offen sc1\n" \
    "s_add_u32 s51, s41, s61\n" \
    "s_add_u32 s61, s61, 524288\n" \
    "s_cmp_eq_u32 s61, 1572864\n" \
    "s_cmov_b32 s61, 0\n" \
    "s_add_u32 s73, s61, 0x40000\n" \
    "s_add_u32 s68, s40, s61\n" \
    "s_add_u32 s78, s78, 16384\n" \
    "s_add_u32 s79, s79, 4\n" \
    "v_mov_b64 v[52:53], 0\n" \
    "v_mov_b64 v[54:55], 0\n" \
    "v_mov_b64 v[56:57], 0\n" \
    "v_mov_b64 v[58:59], 0\n" \
    "v_mov_b64 v[60:61], 0\n" \
    "v_mov_b64 v[62:63], 0\n" \
    "v_mov_b64 v[64:65], 0\n" \
    "v_mov_b64 v[66:67], 0\n" \
    "v_mov_b64 v[100:101], 0\n" \
    "v_mov_b64 v[102:103], 0\n" \
    "v_mov_b64 v[104:105], 0\n" \
    "v_mov_b64 v[106:107], 0\n" \
    "v_mov_b64 v[108:109], -1\n" \
    "v_mov_b64 v[110:111], -1\n" \
    "s_waitcnt lgkmcnt(0)\n" \
    "s_mov_b32 s71, 131072\n" \
    "s_add_u32 s60, s60, 1\n" \
    "s_branch LCP2%=\n" \
      \
    "LCSTEP0%=:\n" \
    "s_nop 1\n" \
    "v_smfmac_f32_16x16x64_bf16 v[52:55], v[20:23], v[28:35], v1\n" \
    "v_smfmac_f32_16x16x64_bf16 v[56:59], v[20:23], v[36:43], v1\n" \
    "v_smfmac_f32_16x16x64_bf16 v[60:63], v[20:23], v[44:51], v1\n" \
    "v_smfmac_f32_16x16x64_bf16 v[64:67], v[20:23], v[116:123], v1\n" \
    "s_branch LCSTEP%=\n" \
    "LCP2R%=:\n" \
    "s_nop 7\n" \
    "v_mov_b64 v[52:53], 0\n" \
    "v_mov_b64 v[54:55], 0\n" \
    "v_mov_b64 v[56:57], 0\n" \
    "v_mov_b64 v[58:59], 0\n" \
    "v_mov_b64 v[60:61], 0\n" \
    "v_mov_b64 v[62:63], 0\n" \
    "v_mov_b64 v[64:65], 0\n" \
    "v_mov_b64 v[66:67], 0\n" \
    "s_sub_u32 s71, s71, 1\n" \
    "s_cbranch_scc1 LCEND%=\n" \
    "buffer_load_dwordx4 v[20:23], v0, s[52:55], s69 offen sc1\n" \
    "s_waitcnt vmcnt(0)\n" \
    "s_branch LCP2b%=\n" \
    "LCP3R%=:\n" \
    "s_sub_u32 s71, s71, 1\n" \
    "s_cbranch_scc1 LCEND%=\n" \
    "buffer_load_dwordx4 v[24:27], v0, s[52:55], s75 offen sc1\n" \
    "s_waitcnt vmcnt(0)\n" \
    "v_max3_u32 v116, v24, v25, v26\n" \
    "v_max_u32 v116, v116, v27\n" \
    "v_cmp_eq_u32 vcc, -1, v116\n" \
    "s_cbranch_vccnz LCP3R%=\n" \
    "ds_write_b128 v2, v[24:27] offset:4096\n" \
    "s_branch LCP3c%=\n" \
    "LCKVSLOW%=:\n" \
    "s_mov_b32 s71, 131072\n" \
    "LCKV1%=:\n" \
    "s_sleep 2\n" \
    "buffer_load_dword v17, off, s[56:59], s79 sc1\n" \
    "s_sub_u32 s71, s71, 1\n" \
    "s_cbranch_scc1 LCEND%=\n" \
    "s_waitcnt vmcnt(0)\n" \
    "v_readfirstlane_b32 s72, v17\n" \
    "s_cmp_lt_u32 s72, 4\n" \
    "s_cbranch_scc1 LCKV1%=\n" \
    "s_branch LCKVOK%=\n" \
    "LCLAST%=:\n" \
    "s_waitcnt lgkmcnt(0)\n" \
    "s_nop 7\n" \
    "v_pk_add_f32 v[100:101], v[100:101], v[102:103]\n" \
    "v_pk_add_f32 v[104:105], v[104:105], v[106:107]\n" \
    "v_cvt_pk_bf16_f32 v100, v100, v101\n" \
    "v_cvt_pk_bf16_f32 v101, v104, v105\n" \
    "v_add_u32 v126, s74, v7\n" \
    "ds_read_b64_tr_b16 v[124:125], v126\n" \
    "ds_read_b64_tr_b16 v[126:127], v126 offset:512\n" \
    "buffer_store_dwordx2 v[100:101], v15, s[56:59], s67 offen sc1\n" \
    "s_waitcnt lgkmcnt(0)\n" \
    "buffer_store_dwordx4 v[124:127], v14, s[56:59], s66 offen sc1\n" \
    "s_waitcnt vmcnt(0)\n" \
    "s_barrier\n" \
    "s_cmp_lg_u32 s47, 3\n" \
    "s_cbranch_scc1 LCEND%=\n" \
    "v_mov_b32 v17, 1\n" \
    "s_mov_b64 exec, 1\n" \
    "buffer_store_dword v17, off, s[56:59], s77 sc1\n" \
    "s_add_u32 s77, s77, 128\n" \
    "buffer_store_dword v17, off, s[56:59], s77 sc1\n" \
    "s_mov_b64 exec, -1\n" \
    "LCEND%=:\n" \
    "s_waitcnt vmcnt(0) lgkmcnt(0)\n" \
    "s_nop 7\n"

#define CHAIN_CLOBBERS "v18", "v19", "v20", "v21", "v22", "v23", "v24", "v25", "v26", "v27", "v36", "v37", "v38", "v39", "v40", "v41", "v42", "v43", "v44", "v45", "v46", "v47", "v48", "v49", "v50", "v51", "v52", "v53", "v54", "v55", "v56", "v57", "v58", "v59", "v60", "v61", "v62", "v63", "v64", "v65", "v66", "v67", "v68", "v69", "v70", "v71", "v72", "v73", "v74", "v75", "v76", "v77", "v78", "v79", "v80", "v81", "v82", "v83", "v84", "v85", "v86", "v87", "v88", "v89", "v90", "v91", "v92", "v93", "v94", "v95", "v96", "v97", "v98", "v99", "v100", "v101", "v102", "v103", "v104", "v105", "v106", "v107", "v108", "v109", "v110", "v111", "v112", "v113", "v114", "v115", "v116", "v117", "v118", "v119", "v120", "v121", "v122", "v123", "v124", "v125", "v126", "v127", "v128", "v129", "v130", "v131", "v132", "v133", "v134", "v135", "v136", "v137", "v138", "v139", "v140", "v141", "v142", "v143", "v144", "v145", "v146", "v147", "v148", "v149", "v150", "v151", "v152", "v153", "v154", "v155", "v156", "v157", "v158", "v159", "v160", "v161", "v162", "v163", "v164", "v165", "v166", "v167", "v168", "v169", "v170", "v171", "v172", "v173", "v174", "v175", "v176", "v177", "v178", "v179", "v180", "v181", "v182", "v183", "v184", "v185", "v186", "v187", "v188", "v189", "v190", "v191", "v192", "v193", "v194", "v195", "v196", "v197", "v198", "v199", "v200", "v201", "v202", "v203", "v204", "v205", "v206", "v207", "v208", "v209", "v210", "v211", "v212", "v213", "v214", "v215", "v216", "v217", "v218", "v219", "v220", "v221", "v222", "v223", "v224", "v225", "v226", "v227", "v228", "v229", "v230", "v231", "v232", "v233", "v234", "v235", "v236", "v237", "v238", "v239", "v240", "v241", "v242", "v243", "v244", "v245", "v246", "v247", "v248", "v249", "v250", "v251", "v252", "v253", "v254", "v255", "a0", "a1", "a2", "a3", "a4", "a5", "a6", "a7", "a8", "a9", "a10", "a11", "a12", "a13", "a14", "a15", "a16", "a17", "a18", "a19", "a20", "a21", "a22", "a23", "a24", "a25", "a26", "a27", "a28", "a29", "a30", "a31", "a32", "a33", "a34", "a35", "a36", "a37", "a38", "a39", "a40", "a41", "a42", "a43", "a44", "a45", "a46", "a47", "a48", "a49", "a50", "a51", "a52", "a53", "a54", "a55", "a56", "a57", "a58", "a59", "a60", "a61", "a62", "a63", "a64", "a65", "a66", "a67", "a68", "a69", "a70", "a71", "a72", "a73", "a74", "a75", "a76", "a77", "a78", "a79", "a80", "a81", "a82", "a83", "a84", "a85", "a86", "a87", "a88", "a89", "a90", "a91", "a92", "a93", "a94", "a95", "a96", "a97", "a98", "a99", "a100", "a101", "a102", "a103", "a104", "a105", "a106", "a107", "a108", "a109", "a110", "a111", "a112", "a113", "a114", "a115", "a116", "a117", "a118", "a119", "a120", "a121", "a122", "a123", "a124", "a125", "a126", "a127", "a128", "a129", "a130", "a131", "a132", "a133", "a134", "a135", "a136", "a137", "a138", "a139", "a140", "a141", "a142", "a143", "a144", "a145", "a146", "a147", "a148", "a149", "a150", "a151", "a152", "a153", "a154", "a155", "a156", "a157", "a158", "a159", "a160", "a161", "a162", "a163", "a164", "a165", "a166", "a167", "a168", "a169", "a170", "a171", "a172", "a173", "a174", "a175", "a176", "a177", "a178", "a179", "a180", "a181", "a182", "a183", "a184", "a185", "a186", "a187", "a188", "a189", "a190", "a191", "a192", "a193", "a194", "a195", "a196", "a197", "a198", "a199", "a200", "a201", "a202", "a203", "a204", "a205", "a206", "a207", "a208", "a209", "a210", "a211", "a212", "a213", "a214", "a215", "a216", "a217", "a218", "a219", "a220", "a221", "a222", "a223", "a224", "a225", "a226", "a227", "a228", "a229", "a230", "a231", "a232", "a233", "a234", "a235", "a236", "a237", "a238", "a239", "a240", "a241", "a242", "a243", "a244", "a245", "a246", "a247", "a248", "a249", "a250", "a251", "a252", "a253", "a254", "a255", "s52", "s53", "s54", "s55", "s56", "s57", "s58", "s59", "s60", "s61", "s62", "s63", "s64", "s65", "s66", "s67", "s68", "s69", "s70", "s71", "s72", "s73", "s74", "s75", "s76", "s77", "s78", "s79", "s80", "s81", "s82", "s83", "s84", "s85", "s86", "s87", "s88", "s89", "s90", "s91", "s92", "s93", "s94", "s95", "s96", "s97", "s98", "s99", "vcc", "scc", "memory"
constexpr int C_KX = 131072, C_MEM = 139264, C_QK = 147456, C_OI = 151552, C_DUMMY = 153600;
constexpr float SM_C = 0.08838834764831845f * 1.4426950408889634f;
typedef unsigned u8v __attribute__((ext_vector_type(8)));
typedef unsigned u16v __attribute__((ext_vector_type(16)));

DEVI void chain_role(const Params& p, unsigned lb, int h, int w, int lane) {
    const unsigned n = lane & 15, g = lane >> 4, l16 = lane * 16;
    u16v vi;
    vi[0] = l16;
    vi[1] = (lane & 2) ? 0xEEEE : 0x4444;
    vi[2] = lb + C_MEM + w * 1024 + l16;
    vi[3] = lb + C_MEM + (w ^ 1) * 1024 + l16; vi[4] = lb + C_MEM + (w ^ 2) * 1024 + l16; vi[5] = lb + C_MEM + (w ^ 3) * 1024 + l16;
    vi[6] = lb + C_QK + (32 * w + n) * 32 + 8 * (g ^ (n >> 2));
    vi[7] = lb + C_QK + (lane >> 2) * 32 + 8 * ((lane & 3) ^ g);
    vi[8] = lb + C_KX + l16;
    vi[9] = (n & 2) ? lb + C_OI + (w >> 1) * 1024 + (32 * (w & 1) + 16 * (g >> 1) + 4 * (n >> 2) + 2 * (g & 1) + (n & 1)) * 16
                    : lb + C_DUMMY + (16 * (g >> 1) + 4 * (n >> 2) + 2 * ((g & 1) ^ 1) + (n & 1)) * 16;
    vi[10] = lb + C_OI + l16;
    vi[11] = lb + w * 32768 + l16;
    vi[12] = (4 * w + (n >> 2)) * 16384 + (g >> 1) * 4096 + h * 128 + (g & 1) * 64 + (n & 3) * 16;
    { const unsigned ch = 8 * (w & 1) + (n & 7);
      vi[13] = (n < 8 && g < 2) ? h * 256 + (4 * (ch >> 2) + 2 * (ch & 1) + g) * 16 + ((ch >> 1) & 1) * 8 : 0x80000000u; }
    vi[14] = (n & 2) ? 0x80000000u : g * 128 + (2 * (n >> 2) + (n & 1)) * 16;
    vi[15] = lane * 8;
    u8v st;
    unsigned v17 = lb + C_KX + ((2 * (n >> 2) + (n & 1)) * 512 + 64 * w + 4 * g + ((n >> 1) & 1)) * 2;
    {
        st[0] = (16 * (g & 1) + (g >> 1)) * 8192 + ((n ^ (g << 1)) * 16); st[1] = st[0] ^ 128;
        { const unsigned c0 = n ^ (g << 1), c1 = c0 ^ 8, rb = (4 * (g >> 1) + (g & 1)) * 1024;
          st[2] = rb + (c0 & 7) * 16 + (c0 >> 3) * 512; st[3] = rb + (c1 & 7) * 16 + (c1 >> 3) * 512; }
        st[4] = lb + (lane >> 2) * 256 + (((2 * w) ^ ((lane >> 2) & 7)) * 32) + 8 * (lane & 3);
        st[5] = lb + w * 32768 + (lane >> 2) * 256 + (((lane >> 2) & 7) * 32) + 8 * (lane & 3);
        st[6] = 0; st[7] = 0;
    }
    u16v si;
    auto put = [&](int i, const void* q) { const uint64_t a = (uint64_t)q; si[i] = (unsigned)a; si[i + 1] = (unsigned)(a >> 32); };
    put(0, p.ws);
    put(2, p.wq + h * 256); put(4, p.wk + h * 256); put(6, p.wv + h * 256);
    put(8, p.wo + (size_t)h * 128 * 1024 + w * 128);
    si[10] = h; si[11] = w; put(12, p.mem0); si[14] = lb; si[15] = (h < 16 ? p.d1 & 0xff : (p.d1 >> 8) & 0xff) | ((p.d1 >> 16) << 8) | ((h < 16 ? p.d2 & 0xff : p.d2 >> 8) << 16);
#pragma unroll
    for (int i = 0; i < 16; ++i) si[i] = __builtin_amdgcn_readfirstlane(si[i]);
    asm volatile(CHAIN_ASM : "+{v17}"(v17), "+{v[28:35]}"(st), "+{s[36:51]}"(si) : "{v[0:15]}"(vi) : CHAIN_CLOBBERS);
}
DEVI bool attn_job(const Params& p, rsrc_t rw, int s, int h, int lane) {
    const int n = lane & 15, g = lane >> 4;
    const unsigned sh = s * 32 + h, l16 = lane * 16;
    GAS unsigned* f3 = lane < 2 ? flagp(p, lane == 0 ? F_KV + h * 64 + s : F_Q + s * 16 + (h >> 1)) : (GAS unsigned*)(p.ws + kmf_off(s, h));
    unsigned long long miss;
    for (int sp = 0;; ++sp) {
        miss = __builtin_amdgcn_ballot_w64(__hip_atomic_load(f3, __ATOMIC_RELAXED, __HIP_MEMORY_SCOPE_AGENT) + (lane < 2 ? 0u : 1u) < (lane < 2 ? 4u : 1u));
        if ((miss & 3) == 0) break;
        if (sp > SPIN_CAP) return false;
        __builtin_amdgcn_s_sleep(8);
    }
    asm volatile("" ::: "memory");
    u4 q[2][4], k[2][4], v[8];
#pragma unroll
    for (int t = 0; t < 4; ++t) {
        q[0][t] = ldw(rw, l16 + t * 1024, WS_QX + sh * 8192); q[1][t] = ldw(rw, l16 + t * 1024, WS_QX + sh * 8192 + 4096);
        k[0][t] = ldw(rw, l16 + t * 1024, kx_off(s, h)); k[1][t] = ldw(rw, l16 + t * 1024, kx_off(s, h) + 4096);
    }
#pragma unroll
    for (int t = 0; t < 8; ++t) v[t] = ldw(rw, l16 + (t & 3) * 1024, kx_off(s, h) + 8192 + (t >> 2) * 4096);
    f4 s00 = {}, s10 = {}, s11 = {}, sm0 = {}, sm1 = {};
    if (miss != 0) {
#pragma unroll
        for (int t = 0; t < 4; ++t) { s00 = mm32(asbf(k[0][t]), asbf(q[0][t]), s00); s10 = mm32(asbf(k[0][t]), asbf(q[1][t]), s10); s11 = mm32(asbf(k[1][t]), asbf(q[1][t]), s11); }
        SB();
        if (!wait_km(p, s, h, 0)) return false;
    }
    u4 km[4]; u2 vmm[4];
#pragma unroll
    for (int t = 0; t < 4; ++t) km[t] = ldw(rw, g * 128 + (2 * (n >> 2) + (n & 1)) * 16 + t * 512, km_off(s, h));
#pragma unroll
    for (int t = 0; t < 4; ++t) vmm[t] = __builtin_amdgcn_raw_buffer_load_b64(rw, lane * 8 + t * 512, km_off(s, h) + 2048, 16);
    SB();
    if (miss == 0) {
#pragma unroll
        for (int t = 0; t < 4; ++t) { s00 = mm32(asbf(k[0][t]), asbf(q[0][t]), s00); s10 = mm32(asbf(k[0][t]), asbf(q[1][t]), s10); s11 = mm32(asbf(k[1][t]), asbf(q[1][t]), s11); }
    }
#pragma unroll
    for (int t = 0; t < 4; ++t) { sm0 = mm32(asbf(km[t]), asbf(q[0][t]), sm0); sm1 = mm32(asbf(km[t]), asbf(q[1][t]), sm1); }
    float m0 = fmaxf(sm0[0], sm0[1]), m1 = fmaxf(sm1[0], sm1[1]);
#pragma unroll
    for (int r = 0; r < 4; ++r) {
        if (4 * g + r > n) { s00[r] = -INFINITY; s11[r] = -INFINITY; }
        m0 = fmaxf(m0, s00[r]); m1 = fmaxf(m1, fmaxf(s10[r], s11[r]));
    }
    m0 = xmax32(xmax16(m0)); m1 = xmax32(xmax16(m1));
    const float c0 = -m0 * SM_C, c1 = -m1 * SM_C;
#pragma unroll
    for (int r = 0; r < 4; ++r) {
        s00[r] = __builtin_amdgcn_exp2f(fmaf(s00[r], SM_C, c0));
        s10[r] = __builtin_amdgcn_exp2f(fmaf(s10[r], SM_C, c1)); s11[r] = __builtin_amdgcn_exp2f(fmaf(s11[r], SM_C, c1));
    }
    const unsigned pm0 = pk2(__builtin_amdgcn_exp2f(fmaf(sm0[0], SM_C, c0)), __builtin_amdgcn_exp2f(fmaf(sm0[1], SM_C, c0)));
    const unsigned pm1 = pk2(__builtin_amdgcn_exp2f(fmaf(sm1[0], SM_C, c1)), __builtin_amdgcn_exp2f(fmaf(sm1[1], SM_C, c1)));
    const bf8 p0 = asbf((u4){pk2(s00[0], s00[1]), pk2(s00[2], s00[3]), pm0, 0u});
    const bf8 p1 = asbf((u4){pk2(s10[0], s10[1]), pk2(s10[2], s10[3]), pk2(s11[0], s11[1]), pk2(s11[2], s11[3])});
    const bf8 p1m = asbf((u4){pm1, 0u, 0u, 0u});
    const u4 one = {0x3f803f80u, 0x3f803f80u, 0x3f803f80u, 0x3f803f80u};
    const f4 l0 = mm32(asbf(one), p0, (f4){});
    const f4 l1 = mm32(asbf(one), p1m, mm32(asbf(one), p1, (f4){}));
    const float r0 = __builtin_amdgcn_rcpf(l0[0]), r1 = __builtin_amdgcn_rcpf(l1[0]);
#pragma unroll
    for (int tp = 0; tp < 4; ++tp) {
        f4 o[2][2];
        const u2 vm = vmm[tp];
#pragma unroll
        for (int c = 0; c < 2; ++c) {
            const u4 vv = v[2 * tp + c];
            o[0][c] = mm32(asbf((u4){vv[0], vv[1], vm[c], 0u}), p0, (f4){}) * r0;
            o[1][c] = mm32(asbf((u4){vm[c], 0u, 0u, 0u}), p1m, mm32(asbf(vv), p1, (f4){})) * r1;
        }
#pragma unroll
        for (int i = 0; i < 2; ++i)
            stw((u4){pk2(o[i][0][0], o[i][0][1]), pk2(o[i][0][2], o[i][0][3]), pk2(o[i][1][0], o[i][1][1]), pk2(o[i][1][2], o[i][1][3])}, rw, l16 + tp * 1024,
                WS_O + s * (2 * 128 * 1024) + h * 4096 + i * (128 * 1024));
    }
    WAIT_VM(0);
    BAR();
    if (threadIdx.x < 2) __hip_atomic_fetch_add((GAS unsigned*)(p.ws + WS_OC + s * 1024 + (h >> 3) * 256 + lane * 128), 1u, __ATOMIC_RELAXED, __HIP_MEMORY_SCOPE_AGENT);
    return true;
}

DEVI void attn_role(const Params& p, rsrc_t rw, int h, int s0, int ds, int lane) {
    for (int s = s0; s < NSEG; s += ds) if (!attn_job(p, rw, s, h, lane)) return;
}

DEVI void down_role(const Params& p, rsrc_t rw, lptr L, int cb, int s0, int ds, int w, int lane) {
    bf8 B[4][14];
    {
        int b[4];
#pragma unroll
        for (int c = 0; c < 4; ++c) b[c] = tr_base<128>(c, lane);
        auto off = [&](int io, int cs) { return io * 1024 + cs * 16; };
        load_panel<128, 7>(mkrsrc(p.wd + (size_t)w * 448 * 1024 + cb * 128, 448 * 1024), off, L + w * 32768, lane, [&](int u, lptr U) {
#pragma unroll
            for (int t = 0; t < 2; ++t)
#pragma unroll
                for (int c = 0; c < 4; ++c) B[c][2 * u + t] = cat2(trd(U + b[c] + t * 4096), trd(U + b[c] + t * 4096 + 2048));
        });
    }
    BAR();
    int it = 0;
    for (int s = s0; s < NSEG; s += ds, it ^= 1) {
        if (!wait_flag(p, F_O + s * 32 + 1, 56)) return;
        const unsigned A = WS_H + s * (2 * 56 * 1024) + w * 14 * 1024;
        lptr X = L + it * 32768;
        u4 a[2][14];
#pragma unroll
        for (int i = 0; i < 2; ++i)
#pragma unroll
            for (int t = 0; t < 14; ++t) a[i][t] = ldw(rw, lane * 16 + t * 1024, A + i * (56 * 1024));
        SB();
#pragma unroll
        for (int i = 0; i < 2; ++i) {
            f4 acc[4] = {};
#pragma unroll
            for (int t = 0; t < 14; ++t)
#pragma unroll
                for (int c = 0; c < 4; ++c) acc[c] = mm32(B[c][t], asbf(a[i][t]), acc[c]);
#pragma unroll
            for (int c = 0; c < 4; ++c) *(LAS f4*)(X + w * 8192 + (i * 4 + c) * 1024 + lane * 16) = acc[c];
        }
        LBAR();
#pragma unroll
        for (int i = 0; i < 2; ++i) {
            f4 sum = {};
#pragma unroll
            for (int k = 0; k < 4; ++k) sum += *(LAS f4*)(X + k * 8192 + (i * 4 + w) * 1024 + lane * 16);
            float o[4];
#pragma unroll
            for (int r = 0; r < 4; ++r) { const float z = bfr(sum[r]); o[r] = z / (1.f + __expf(-z)); }
            __builtin_amdgcn_raw_buffer_store_b64((u2){pk2(o[0], o[1]), pk2(o[2], o[3])}, rw, (lane & 15) * 1024 + (lane >> 4) * 8, WS_G + (s * 32 + i * 16) * 1024 + cb * 128 + w * 32, 16);
        }
        WAIT_VM(0);
        add_flag(p, F_O + s * 32 + 8 + cb, lane);
    }
}

DEVI void wo_role(const Params& p, rsrc_t rw, lptr L, int cb, int ti, int s0, int ds, int w, int lane) {
    bf8 B[2][32];
    {
        const int b0 = tr_base<64>(0, lane), b1 = tr_base<64>(1, lane);
        auto off = [&](int io, int cs) { return (io ^ (cb * 64)) * 1024 + cs * 16; };
        load_panel<64, 8>(mkrsrc(p.wo + (size_t)w * 1024 * 1024 + cb * 64, 1024 * 1024), off, L + w * 32768, lane, [&](int u, lptr U) {
#pragma unroll
            for (int t = 0; t < 4; ++t) {
                B[0][u * 4 + t] = cat2(trd(U + b0 + t * 2048), trd(U + b0 + t * 2048 + 1024));
                B[1][u * 4 + t] = cat2(trd(U + b1 + t * 2048), trd(U + b1 + t * 2048 + 1024));
            }
        });
    }
    if (ds != 2 && w == 0 && !wait_km(p, 58, cb, 1)) return;
    BAR();
    const unsigned fo = lane == 0 ? COMM_CTRL + (F_O + 8 + (cb >> 1)) * 4 : WS_OC + w * 256 + ti * 128, fs = lane == 0 ? 128 : 1024, want = lane == 0 ? 4 : 2;
    const unsigned eo = (ti * 16 + (lane & 15)) * 1024 + cb * 64 + (w & 1) * 32 + (lane >> 4) * 8;
    const rsrc_t rx = mkrsrc(p.x, 2u << 20);
    u4 a[32]; u2 xx, gx;
    auto request = [&](int s) {
        xx = __builtin_amdgcn_raw_buffer_load_b64(rx, eo, s * 32768, 0);
        gx = __builtin_amdgcn_raw_buffer_load_b64(rw, eo, WS_G + s * 32768, 16);
        const unsigned A = WS_O + s * (2 * 128 * 1024) + ti * (128 * 1024) + w * 32 * 1024;
#pragma unroll
        for (int j = 0; j < 32; ++j) a[j] = ldw(rw, lane * 16 + (j & 1) * 1024, A + (((j >> 1) ^ cb) * 2048));
    };
    bool have = false;
    int it = 0;
    for (int s = s0; s < NSEG; it ^= 1) {
        int sn = s + ds;
        if (sn == 61 && ds == 2) sn = 63;
        if (!have) {
            for (int sp = 0;; ++sp) {
                asm volatile("" ::: "memory");
                if (__builtin_amdgcn_ballot_w64(__builtin_amdgcn_raw_buffer_load_b32(rw, fo + s * fs, 0, 16) < want) == 0) break;
                if (sp > SPIN_CAP) return;
                __builtin_amdgcn_s_sleep(3);
            }
            asm volatile("" ::: "memory");
            request(s);
        }
        const unsigned fn = __builtin_amdgcn_raw_buffer_load_b32(rw, fo + min(sn, NSEG - 1) * fs, 0, 16);
        SB();
        f4 acc[2][2] = {};
#pragma unroll
        for (int j = 0; j < 32; ++j) { acc[j & 1][0] = mm32(B[0][j], asbf(a[j]), acc[j & 1][0]); acc[j & 1][1] = mm32(B[1][j], asbf(a[j]), acc[j & 1][1]); }
        const u2 x0 = xx, g0 = gx;
        lptr X = L + it * 8192;
        *(LAS f4*)(X + w * 2048 + lane * 16) = acc[0][0] + acc[1][0];
        *(LAS f4*)(X + w * 2048 + 1024 + lane * 16) = acc[0][1] + acc[1][1];
        have = sn < NSEG && __builtin_amdgcn_ballot_w64(fn < want) == 0;
        asm volatile("" ::: "memory");
        if (have) request(sn);
        LBAR();
        if (w < 2) {
            f4 sum = {};
#pragma unroll
            for (int k = 0; k < 4; ++k) sum += *(LAS f4*)(X + k * 2048 + w * 1024 + lane * 16);
            const float a0 = bflo(x0[0]) + bfr(bfr(sum[0]) * bflo(g0[0])), a1 = bfhi(x0[0]) + bfr(bfr(sum[1]) * bfhi(g0[0]));
            const float a2 = bflo(x0[1]) + bfr(bfr(sum[2]) * bflo(g0[1])), a3 = bfhi(x0[1]) + bfr(bfr(sum[3]) * bfhi(g0[1]));
            *(u2*)(p.out + (size_t)s * 32768 + eo) = (u2){pk2(a0, a1), pk2(a2, a3)};
        }
        s = sn;
    }
}

__global__ void __launch_bounds__(256, 1) k_all(Params p) {
    extern __shared__ __attribute__((aligned(16))) char smem[];
    const int w = __builtin_amdgcn_readfirstlane(threadIdx.x >> 6), lane = threadIdx.x & 63;
    const int b = blockIdx.x, x = b & 7, r = b >> 3;
    lptr L = (lptr)smem;
    const bool cx = x < 2;
    if (cx && r < 16) { chain_role(p, (unsigned)(uintptr_t)L, r * 2 + x, w, lane); return; }
    const u64 tag = __hip_atomic_load(rootp(p, 0), __ATOMIC_RELAXED, __HIP_MEMORY_SCOPE_AGENT) + 1;
    const rsrc_t rw = mkrsrc(p.ws, WS_END);
    const int c = x - 2;
    int ta = 999, li = -1, tb = -1, ti = 0;
    const int si = (r - 16) * 2 + x;
    if (cx) li = si;
    else if (r < 11) { ta = c + 6 * r; if (ta < 64) { tb = 1 + (ta >> 5); ti = ta & 31; } else ta = 999; }
    else if (c < 4 && r < 27) { tb = 0; ti = c * 16 + (r - 11); li = 32 + ti; }
    else { ta = 64 + (c < 4 ? c * 5 + (r - 27) : 20 + (c - 4) * 21 + (r - 11)); if (ta < 96) li = 32 + ta;
           if (c >= 4 && r < 27) { tb = 0; ti = 64 + (c - 4) * 16 + (r - 11); } }
    if (li >= 0) {
        if (cx) for (int q = si * 4 + w; q != 11 || w == 2; q = 11) {
#pragma unroll
            for (int i = 0; i < 20; ++i) {
                const unsigned o = q * 20480 + (i >> 2) * 4096;
                stw((u4){~0u, ~0u, ~0u, ~0u}, rw, lane * 16 + (i & 3) * 1024, WS_COMM + o + (o >= 96 * 16384 ? 32 * 16384 : 0));
            }
#pragma unroll
            for (int i = 0; i < 3; ++i) stw((u4){~0u, ~0u, ~0u, ~0u}, rw, i == 2 && lane >= 32 ? 0x80000000u : lane * 16 + i * 1024, WS_KMF + q * 2560);
            stw((u4){0u, 0u, 0u, 0u}, rw, lane < 16 ? lane * 16 : 0x80000000u, COMM_CTRL + q * 256);
            stw((u4){0u, 0u, 0u, 0u}, rw, lane < 32 ? lane * 16 : 0x80000000u, WS_OC + q * 512);
            WAIT_VM(0);
            if (lane == 0) __hip_atomic_store(rootp(p, 2048 + 8 * q), tag, __ATOMIC_RELAXED, __HIP_MEMORY_SCOPE_AGENT);
            if (q != 10) break;
        }
        if (cx && si == 2 && w == 3) {
            unsigned best[4] = {~0u, ~0u, ~0u, ~0u};
            for (int it = 0; it < 16; ++it) {
                const char* a = p.ws + WS_COMM + ((it & 2) ? 191 * 16384 + 2048 : 100 * 16384) + (it & 1) * 8192 + ((it >> 2) & 1) * 4096;
                unsigned v, z = 0;
                const unsigned long long c0 = __builtin_amdgcn_s_memtime();
                if (lane == 0) asm volatile("global_atomic_or %0, %1, %2, off sc0\n\ts_waitcnt vmcnt(0)" : "=&v"(v) : "v"(a), "v"(z) : "memory");
                const unsigned dt = (unsigned)(__builtin_amdgcn_s_memtime() - c0);
#pragma unroll
                for (int k = 0; k < 4; ++k) if ((it & 3) == k) best[k] = min(best[k], dt);
            }
            if (lane == 0) __hip_atomic_store(rootp(p, 1152), (tag << 3) | (min(best[1], best[2]) < min(best[0], best[3]) ? 1u : 4u), __ATOMIC_RELAXED, __HIP_MEMORY_SCOPE_AGENT);
        }
        if (cx && si == 0 && w == 3) {
            for (int sp = 0;; ++sp) {
                const u64 a = __hip_atomic_load(rootp(p, 2048 + 8 * lane), __ATOMIC_RELAXED, __HIP_MEMORY_SCOPE_AGENT);
                const u64 e = __hip_atomic_load(rootp(p, 2560 + 8 * lane), __ATOMIC_RELAXED, __HIP_MEMORY_SCOPE_AGENT);
                if (__builtin_amdgcn_ballot_w64(a != tag || e != tag) == 0) break;
                if (sp > SPIN_CAP) return;
                __builtin_amdgcn_s_sleep(4);
            }
            if (lane < 8) __hip_atomic_store(rootp(p, 128 + 128 * lane), tag << 3, __ATOMIC_RELAXED, __HIP_MEMORY_SCOPE_AGENT);
        }
        if (!ln_rows(p, rw, (li * 4 + w) * 4, lane, tag)) return;
        if (cx && si == 0 && w == 3) {
            if (wait_flag(p, 1, 224 + 128) && lane == 0) __hip_atomic_store(rootp(p, 0), tag, __ATOMIC_RELAXED, __HIP_MEMORY_SCOPE_AGENT);
        }
    } else if (ta >= 64 && !wait_go(p, tag, lane)) return;
    if (ta >= 64 && (ta < 124 || tb >= 0)) {
        if (w == 0 && !wait_km(p, ta < 124 ? 0 : 4, b & 31, 1)) return;
        BAR();
    }
    if (ta < 124) {
        const int kind = ta < 64 ? 0 : (ta < 96 ? 1 : 2);
        proj_role(p, rw, L, kind, (ta - (kind == 0 ? 0 : (kind == 1 ? 64 : 96))) >> 1, ta & 1, 2, w, lane, tag);
        LBAR();
    }
    if (tb == 0) wo_role(p, rw, L, ti & 15, (ti >> 4) & 1, ti < 64 ? ti >> 5 : 61, ti < 64 ? 2 : 64, w, lane);
    else if (tb == 1) attn_role(p, rw, (ti & 7) * 4 + w, ti >> 3, 4, lane);
    else if (tb == 2) down_role(p, rw, L, ti & 7, ti >> 3, 4, w, lane);
}

constexpr int g_d1 = 0x020500, g_d2 = 0x0309;

extern "C" void kernel_launch(void* const* d_in, const int* in_sizes, int n_in, void* d_out, int out_size, void* d_ws, size_t ws_size, hipStream_t stream) {
    Params p;
    p.x = (const char*)d_in[0]; p.mem0 = (const char*)d_in[1]; p.wq = (const char*)d_in[2]; p.wk = (const char*)d_in[3];
    p.wv = (const char*)d_in[4]; p.wo = (const char*)d_in[5]; p.wg = (const char*)d_in[8]; p.wu = (const char*)d_in[9]; p.wd = (const char*)d_in[10];
    p.out = (char*)d_out;
    p.ws = (char*)(((uintptr_t)d_ws + ((2u << 20) - 1)) & ~(uintptr_t)((2u << 20) - 1));
    p.d1 = g_d1; p.d2 = g_d2;
    (void)hipFuncSetAttribute((const void*)k_all, hipFuncAttributeMaxDynamicSharedMemorySize, LDS_BYTES);
    k_all<<<256, 256, LDS_BYTES, stream>>>(p);
}
```
